```python
import torch

def _softmax(x, axis=-1):
    return torch.softmax(x, dim=axis)
def _gelu(x):
    return torch.nn.functional.gelu(x, approximate="tanh")
def _silu(x):
    return torch.nn.functional.silu(x)
def _sigmoid(x):
    return torch.sigmoid(x)
def _layernorm(x, axis=-1, eps=1e-5):
    m = x.mean(dim=axis, keepdim=True)
    v = x.var(dim=axis, keepdim=True, unbiased=False)
    return (x - m) / torch.sqrt(v + eps)
def _scatter_add(tgt, idx, src, axis=0):
    return tgt.clone().index_add_(axis, idx, src)
def _segment_sum(src, seg, n):
    out = torch.zeros((n,) + src.shape[1:], dtype=src.dtype)
    return out.index_add_(0, seg, src)
def _pad(x, pw):
    flat = tuple(v for pair in reversed(pw) for v in pair)
    return torch.nn.functional.pad(x, flat)

kernel_name = 'sequential_preLN__spectral_gfnet__geglu_ffn__prompt'

def setup_inputs():

    _w = (lambda a: (lambda t: t.to(torch.bfloat16) if t.is_floating_point() else t)(torch.as_tensor(a)))
    def _A_setup():
        B, S, D = 1, 2048, 640
        Df = D // 2 + 1
        return {
            "x": _w((lambda *s: torch.randn(*s, dtype=torch.bfloat16) * 0.05)(B, S, D)),
            "w_r": _w((lambda *s: torch.randn(*s, dtype=torch.bfloat16) * 0.4)(S, Df) * 0.02),
            "w_i": _w((lambda *s: torch.randn(*s, dtype=torch.bfloat16) * 0.4)(S, Df) * 0.02),
        }
    _A_in = _A_setup()

    def _B_setup():
        B, S, D, F = 1, 2048, 640, 1536
        return {
            "x": _w((lambda *s: torch.randn(*s, dtype=torch.bfloat16) * 0.05)(B, S, D)),
            "w_gate": _w((lambda *s: torch.randn(*s, dtype=torch.bfloat16) * 0.05)(D, F)),
            "w_up": _w((lambda *s: torch.randn(*s, dtype=torch.bfloat16) * 0.05)(D, F)),
            "w_down": _w((lambda *s: torch.randn(*s, dtype=torch.bfloat16) * 0.05)(F, D)),
        }
    _B_in = _B_setup()

    return {
        "A_x": _A_in["x"],
        "A_w_r": _A_in["w_r"],
        "A_w_i": _A_in["w_i"],
        "B_x": _B_in["x"],
        "B_w_gate": _B_in["w_gate"],
        "B_w_up": _B_in["w_up"],
        "B_w_down": _B_in["w_down"],
    }

def reference(A_x, A_w_r, A_w_i, B_x, B_w_gate, B_w_up, B_w_down):
    def _A_ref(x, w_r, w_i):
        B, S, D = x.shape
        X = (lambda x, axes=(-2,-1): torch.fft.rfft2(x.float(), dim=axes))(x, axes=(-2, -1))
        W = w_r + 1j * w_i
        X = X * W
        y = (lambda x, s, axes=(-2,-1): torch.fft.irfft2(x, s=s, dim=axes).to(torch.bfloat16))(X, (S, D), axes=(-2, -1))
        return y

    def _B_ref(x, w_gate, w_up, w_down):
        return (_gelu(x @ w_gate) * (x @ w_up)) @ w_down

    x = A_x
    h = x + _A_ref(_layernorm(x, axis=-1), A_w_r, A_w_i)
    h2 = h + _B_ref(_layernorm(h, axis=-1), B_w_gate, B_w_up, B_w_down)
    return h2
```

```cpp
#include <hip/hip_runtime.h>
#include <hip/hip_bf16.h>
#include <stdint.h>
#include <type_traits>

typedef float f2 __attribute__((ext_vector_type(2)));
typedef float f4 __attribute__((ext_vector_type(4)));
typedef short bf16x8 __attribute__((ext_vector_type(8)));
typedef unsigned u4 __attribute__((ext_vector_type(4)));
typedef unsigned u2 __attribute__((ext_vector_type(2)));
typedef __bf16 bfx2 __attribute__((ext_vector_type(2)));

static __device__ const __attribute__((aligned(128))) float TS[4096] = {
0.0f,-1.0f,1.0f,0.0f,1.0f,0.0f,1.0f,0.0f,1.0f,0.0f,1.0f,0.0f,
1.0f,0.0f,1.0f,0.0f,-0.0122715384f,-0.999924719f,0.999995291f,-0.00306795677f,0.999981165f,-0.00613588467f,0.999957621f,-0.00920375437f,
0.999924719f,-0.0122715384f,0.99988234f,-0.015339206f,0.999830604f,-0.0184067301f,0.99976939f,-0.0214740802f,-0.024541229f,-0.999698818f,0.999981165f,-0.00613588467f,
0.999924719f,-0.0122715384f,0.999830604f,-0.0184067301f,0.999698818f,-0.024541229f,0.999529421f,-0.030674804f,0.999322355f,-0.0368072242f,0.999077737f,-0.0429382585f,
-0.0368072242f,-0.999322355f,0.999957621f,-0.00920375437f,0.999830604f,-0.0184067301f,0.999618828f,-0.027608145f,0.999322355f,-0.0368072242f,0.998941302f,-0.0460031815f,
0.998475552f,-0.0551952459f,0.997925282f,-0.0643826276f,-0.0490676761f,-0.99879545f,0.999924719f,-0.0122715384f,0.999698818f,-0.024541229f,0.999322355f,-0.0368072242f,
0.99879545f,-0.0490676761f,0.998118103f,-0.061320737f,0.997290432f,-0.0735645667f,0.996312618f,-0.0857973099f,-0.061320737f,-0.998118103f,0.99988234f,-0.015339206f,
0.999529421f,-0.030674804f,0.998941302f,-0.0460031815f,0.998118103f,-0.061320737f,0.997060061f,-0.0766238645f,0.995767415f,-0.0919089541f,0.994240463f,-0.107172422f,
-0.0735645667f,-0.997290432f,0.999830604f,-0.0184067301f,0.999322355f,-0.0368072242f,0.998475552f,-0.0551952459f,0.997290432f,-0.0735645667f,0.995767415f,-0.0919089541f,
0.993906975f,-0.110222206f,0.991709769f,-0.128498107f,-0.0857973099f,-0.996312618f,0.99976939f,-0.0214740802f,0.999077737f,-0.0429382585f,0.997925282f,-0.0643826276f,
0.996312618f,-0.0857973099f,0.994240463f,-0.107172422f,0.991709769f,-0.128498107f,0.988721669f,-0.149764538f,-0.0980171412f,-0.99518472f,0.999698818f,-0.024541229f,
0.99879545f,-0.0490676761f,0.997290432f,-0.0735645667f,0.99518472f,-0.0980171412f,0.992479563f,-0.122410677f,0.989176512f,-0.146730468f,0.985277653f,-0.170961887f,
-0.110222206f,-0.993906975f,0.999618828f,-0.027608145f,0.998475552f,-0.0551952459f,0.996571124f,-0.0827402622f,0.993906975f,-0.110222206f,0.990485072f,-0.137620121f,
0.986308098f,-0.164913118f,0.981379211f,-0.192080393f,-0.122410677f,-0.992479563f,0.999529421f,-0.030674804f,0.998118103f,-0.061320737f,0.995767415f,-0.0919089541f,
0.992479563f,-0.122410677f,0.988257587f,-0.152797192f,0.983105481f,-0.183039889f,0.977028131f,-0.213110313f,-0.134580702f,-0.990902662f,0.999430597f,-0.0337411724f,
0.997723043f,-0.0674439222f,0.994879305f,-0.10106986f,0.990902662f,-0.134580702f,0.985797524f,-0.167938292f,0.979569793f,-0.201104641f,0.972226501f,-0.234041959f,
-0.146730468f,-0.989176512f,0.999322355f,-0.0368072242f,0.997290432f,-0.0735645667f,0.993906975f,-0.110222206f,0.989176512f,-0.146730468f,0.983105481f,-0.183039889f,
0.975702107f,-0.219101235f,0.966976464f,-0.254865646f,-0.15885815f,-0.987301409f,0.999204755f,-0.0398729257f,0.996820271f,-0.0796824396f,0.992850423f,-0.119365215f,
0.987301409f,-0.15885815f,0.980182111f,-0.198098406f,0.971503913f,-0.237023607f,0.961280465f,-0.275571823f,-0.170961887f,-0.985277653f,0.999077737f,-0.0429382585f,
0.996312618f,-0.0857973099f,0.991709769f,-0.128498107f,0.985277653f,-0.170961887f,0.977028131f,-0.213110313f,0.966976464f,-0.254865646f,0.955141187f,-0.296150893f,
-0.183039889f,-0.983105481f,0.998941302f,-0.0460031815f,0.995767415f,-0.0919089541f,0.990485072f,-0.137620121f,0.983105481f,-0.183039889f,0.973644257f,-0.228072077f,
0.962121427f,-0.272621363f,0.94856137f,-0.316593379f,-0.195090324f,-0.980785251f,0.99879545f,-0.0490676761f,0.99518472f,-0.0980171412f,0.989176512f,-0.146730468f,
0.980785251f,-0.195090324f,0.970031261f,-0.242980182f,0.956940353f,-0.290284663f,0.941544056f,-0.336889863f,-0.207111374f,-0.97831738f,0.998640239f,-0.052131705f,
0.994564593f,-0.104121633f,0.987784147f,-0.155828401f,0.97831738f,-0.207111374f,0.966189981f,-0.257831097f,0.95143503f,-0.307849646f,0.934092522f,-0.357030958f,
-0.219101235f,-0.975702107f,0.998475552f,-0.0551952459f,0.993906975f,-0.110222206f,0.986308098f,-0.164913118f,0.975702107f,-0.219101235f,0.962121427f,-0.272621363f,
0.945607305f,-0.32531029f,0.926210225f,-0.377007425f,-0.231058106f,-0.972939968f,0.998301566f,-0.0582582653f,0.993211925f,-0.116318628f,0.984748483f,-0.173983872f,
0.972939968f,-0.231058106f,0.957826436f,-0.287347466f,0.939459205f,-0.342660725f,0.917900801f,-0.396809995f,-0.242980182f,-0.970031261f,0.998118103f,-0.061320737f,
0.992479563f,-0.122410677f,0.983105481f,-0.183039889f,0.970031261f,-0.242980182f,0.953306019f,-0.302005947f,0.932992816f,-0.359895051f,0.909168005f,-0.416429549f,
-0.254865646f,-0.966976464f,0.997925282f,-0.0643826276f,0.991709769f,-0.128498107f,0.981379211f,-0.192080393f,0.966976464f,-0.254865646f,0.94856137f,-0.316593379f,
0.926210225f,-0.377007425f,0.900015891f,-0.435857087f,-0.266712755f,-0.963776052f,0.997723043f,-0.0674439222f,0.990902662f,-0.134580702f,0.979569793f,-0.201104641f,
0.963776052f,-0.266712755f,0.943593442f,-0.331106305f,0.919113874f,-0.393992037f,0.890448749f,-0.455083579f,-0.27851969f,-0.960430503f,0.997511446f,-0.070504576f,
0.990058184f,-0.140658244f,0.977677345f,-0.210111842f,0.960430503f,-0.27851969f,0.938403547f,-0.345541328f,0.91170603f,-0.410843164f,0.880470872f,-0.474100202f,
-0.290284663f,-0.956940353f,0.997290432f,-0.0735645667f,0.989176512f,-0.146730468f,0.975702107f,-0.219101235f,0.956940353f,-0.290284663f,0.932992816f,-0.359895051f,
0.903989315f,-0.427555084f,0.870086968f,-0.492898196f,-0.302005947f,-0.953306019f,0.997060061f,-0.0766238645f,0.988257587f,-0.152797192f,0.973644257f,-0.228072077f,
0.953306019f,-0.302005947f,0.927362502f,-0.374164075f,0.895966232f,-0.444122136f,0.859301805f,-0.511468828f,-0.313681751f,-0.949528158f,0.996820271f,-0.0796824396f,
0.987301409f,-0.15885815f,0.971503913f,-0.237023607f,0.949528158f,-0.313681751f,0.921514034f,-0.388345033f,0.887639642f,-0.460538715f,0.848120332f,-0.529803634f,
-0.32531029f,-0.945607305f,0.996571124f,-0.0827402622f,0.986308098f,-0.164913118f,0.969281256f,-0.24595505f,0.945607305f,-0.32531029f,0.915448725f,-0.402434647f,
0.879012227f,-0.47679922f,0.836547732f,-0.547894061f,-0.336889863f,-0.941544056f,0.996312618f,-0.0857973099f,0.985277653f,-0.170961887f,0.966976464f,-0.254865646f,
0.941544056f,-0.336889863f,0.909168005f,-0.416429549f,0.870086968f,-0.492898196f,0.824589312f,-0.565731823f,-0.348418683f,-0.937339008f,0.996044695f,-0.0888535529f,
0.984210074f,-0.177004218f,0.964589775f,-0.263754666f,0.937339008f,-0.348418683f,0.902673304f,-0.430326492f,0.860866964f,-0.50883013f,0.812250614f,-0.583308637f,
-0.359895051f,-0.932992816f,0.995767415f,-0.0919089541f,0.983105481f,-0.183039889f,0.962121427f,-0.272621363f,0.932992816f,-0.359895051f,0.895966232f,-0.444122136f,
0.851355195f,-0.524589658f,0.799537241f,-0.600616455f,-0.371317208f,-0.928506076f,0.995480776f,-0.0949634984f,0.981963873f,-0.18906866f,0.95957154f,-0.281464934f,
0.928506076f,-0.371317208f,0.889048338f,-0.457813293f,0.841554999f,-0.540171444f,0.786455214f,-0.61764729f,-0.382683426f,-0.923879504f,0.99518472f,-0.0980171412f,
0.980785251f,-0.195090324f,0.956940353f,-0.290284663f,0.923879504f,-0.382683426f,0.881921291f,-0.471396744f,0.831469595f,-0.555570245f,0.773010433f,-0.634393275f,
-0.393992037f,-0.919113874f,0.994879305f,-0.10106986f,0.979569793f,-0.201104641f,0.954228103f,-0.299079835f,0.919113874f,-0.393992037f,0.874586642f,-0.484869242f,
0.8211025f,-0.570780754f,0.759209216f,-0.65084666f,-0.405241311f,-0.914209783f,0.994564593f,-0.104121633f,0.97831738f,-0.207111374f,0.95143503f,-0.307849646f,
0.914209783f,-0.405241311f,0.867046237f,-0.498227656f,0.81045717f,-0.585797846f,0.745057762f,-0.666999936f,-0.416429549f,-0.909168005f,0.994240463f,-0.107172422f,
0.977028131f,-0.213110313f,0.94856137f,-0.316593379f,0.909168005f,-0.416429549f,0.859301805f,-0.511468828f,0.799537241f,-0.600616455f,0.730562747f,-0.682845533f,
-0.427555084f,-0.903989315f,0.993906975f,-0.110222206f,0.975702107f,-0.219101235f,0.945607305f,-0.32531029f,0.903989315f,-0.427555084f,0.851355195f,-0.524589658f,
0.78834641f,-0.615231574f,0.715730846f,-0.698376238f,-0.438616246f,-0.898674488f,0.993564129f,-0.113270953f,0.974339366f,-0.225083917f,0.94257319f,-0.333999664f,
0.898674488f,-0.438616246f,0.843208253f,-0.537587047f,0.77688849f,-0.629638255f,0.700568795f,-0.71358484f,-0.449611336f,-0.893224299f,0.993211925f,-0.116318628f,
0.972939968f,-0.231058106f,0.939459205f,-0.342660725f,0.893224299f,-0.449611336f,0.834862888f,-0.550457954f,0.765167236f,-0.643831551f,0.685083687f,-0.728464365f,
-0.460538715f,-0.887639642f,0.992850423f,-0.119365215f,0.971503913f,-0.237023607f,0.936265647f,-0.351292759f,0.887639642f,-0.460538715f,0.826321065f,-0.563199341f,
0.753186822f,-0.657806695f,0.669282615f,-0.743007958f,-0.471396744f,-0.881921291f,0.992479563f,-0.122410677f,0.970031261f,-0.242980182f,0.932992816f,-0.359895051f,
0.881921291f,-0.471396744f,0.817584813f,-0.575808167f,0.740951121f,-0.671558976f,0.653172851f,-0.757208824f,-0.482183784f,-0.876070082f,0.992099285f,-0.125454977f,
0.968522072f,-0.248927608f,0.929640889f,-0.368466824f,0.876070082f,-0.482183784f,0.808656156f,-0.588281572f,0.728464365f,-0.685083687f,0.636761844f,-0.771060526f,
-0.492898196f,-0.870086968f,0.991709769f,-0.128498107f,0.966976464f,-0.254865646f,0.926210225f,-0.377007425f,0.870086968f,-0.492898196f,0.799537241f,-0.600616455f,
0.715730846f,-0.698376238f,0.620057225f,-0.784556568f,-0.50353837f,-0.863972843f,0.991310835f,-0.13154003f,0.965394437f,-0.260794103f,0.92270112f,-0.385516047f,
0.863972843f,-0.50353837f,0.790230215f,-0.612810075f,0.702754736f,-0.711432219f,0.603066623f,-0.797690868f,-0.514102757f,-0.857728601f,0.990902662f,-0.134580702f,
0.963776052f,-0.266712755f,0.919113874f,-0.393992037f,0.857728601f,-0.514102757f,0.780737221f,-0.624859512f,0.689540565f,-0.724247098f,0.585797846f,-0.81045717f,
-0.524589658f,-0.851355195f,0.990485072f,-0.137620121f,0.962121427f,-0.272621363f,0.915448725f,-0.402434647f,0.851355195f,-0.524589658f,0.771060526f,-0.636761844f,
0.676092684f,-0.736816585f,0.568258941f,-0.82284981f,-0.534997642f,-0.84485358f,0.990058184f,-0.140658244f,0.960430503f,-0.27851969f,0.91170603f,-0.410843164f,
0.84485358f,-0.534997642f,0.761202395f,-0.64851439f,0.662415802f,-0.749136388f,0.550457954f,-0.834862888f,-0.545324981f,-0.838224709f,0.989621997f,-0.143695027f,
0.958703458f,-0.284407526f,0.907886088f,-0.419216901f,0.838224709f,-0.545324981f,0.751165152f,-0.660114348f,0.64851439f,-0.761202395f,0.532403111f,-0.84649092f,
-0.555570245f,-0.831469595f,0.989176512f,-0.146730468f,0.956940353f,-0.290284663f,0.903989315f,-0.427555084f,0.831469595f,-0.555570245f,0.740951121f,-0.671558976f,
0.634393275f,-0.773010433f,0.514102757f,-0.857728601f,-0.565731823f,-0.824589312f,0.988721669f,-0.149764538f,0.955141187f,-0.296150893f,0.900015891f,-0.435857087f,
0.824589312f,-0.565731823f,0.730562747f,-0.682845533f,0.620057225f,-0.784556568f,0.495565265f,-0.868570685f,-0.575808167f,-0.817584813f,0.988257587f,-0.152797192f,
0.953306019f,-0.302005947f,0.895966232f,-0.444122136f,0.817584813f,-0.575808167f,0.720002532f,-0.693971455f,0.605511069f,-0.795836926f,0.47679922f,-0.879012227f,
-0.585797846f,-0.81045717f,0.987784147f,-0.155828401f,0.95143503f,-0.307849646f,0.891840696f,-0.452349573f,0.81045717f,-0.585797846f,0.709272802f,-0.704934061f,
0.590759695f,-0.806847572f,0.457813293f,-0.889048338f,-0.59569931f,-0.803207517f,0.987301409f,-0.15885815f,0.949528158f,-0.313681751f,0.887639642f,-0.460538715f,
0.803207517f,-0.59569931f,0.698376238f,-0.715730846f,0.575808167f,-0.817584813f,0.438616246f,-0.898674488f,-0.605511069f,-0.795836926f,0.986809373f,-0.161886394f,
0.947585583f,-0.319502026f,0.883363366f,-0.468688816f,0.795836926f,-0.605511069f,0.687315345f,-0.726359129f,0.560661554f,-0.82804507f,0.419216901f,-0.907886088f,
-0.615231574f,-0.78834641f,0.986308098f,-0.164913118f,0.945607305f,-0.32531029f,0.879012227f,-0.47679922f,0.78834641f,-0.615231574f,0.676092684f,-0.736816585f,
0.545324981f,-0.838224709f,0.399624199f,-0.916679084f,-0.624859512f,-0.780737221f,0.985797524f,-0.167938292f,0.943593442f,-0.331106305f,0.874586642f,-0.484869242f,
0.780737221f,-0.624859512f,0.664710999f,-0.747100592f,0.529803634f,-0.848120332f,0.379847199f,-0.925049245f,-0.634393275f,-0.773010433f,0.985277653f,-0.170961887f,
0.941544056f,-0.336889863f,0.870086968f,-0.492898196f,0.773010433f,-0.634393275f,0.653172851f,-0.757208824f,0.514102757f,-0.857728601f,0.359895051f,-0.932992816f,
-0.643831551f,-0.765167236f,0.984748483f,-0.173983872f,0.939459205f,-0.342660725f,0.865513623f,-0.500885367f,0.765167236f,-0.643831551f,0.641481042f,-0.767138898f,
0.498227656f,-0.867046237f,0.339776874f,-0.940506041f,-0.653172851f,-0.757208824f,0.984210074f,-0.177004218f,0.937339008f,-0.348418683f,0.860866964f,-0.50883013f,
0.757208824f,-0.653172851f,0.629638255f,-0.77688849f,0.482183784f,-0.876070082f,0.319502026f,-0.947585583f,-0.662415802f,-0.749136388f,0.983662426f,-0.180022895f,
0.935183525f,-0.354163527f,0.856147349f,-0.516731799f,0.749136388f,-0.662415802f,0.61764729f,-0.786455214f,0.465976506f,-0.884797096f,0.299079835f,-0.954228103f,
-0.671558976f,-0.740951121f,0.983105481f,-0.183039889f,0.932992816f,-0.359895051f,0.851355195f,-0.524589658f,0.740951121f,-0.671558976f,0.605511069f,-0.795836926f,
0.449611336f,-0.893224299f,0.27851969f,-0.960430503f,-0.680601001f,-0.732654274f,0.982539296f,-0.186055154f,0.93076694f,-0.365612984f,0.84649092f,-0.532403111f,
0.732654274f,-0.680601001f,0.593232274f,-0.805031359f,0.433093816f,-0.901348829f,0.257831097f,-0.966189981f,-0.689540565f,-0.724247098f,0.981963873f,-0.18906866f,
0.928506076f,-0.371317208f,0.841554999f,-0.540171444f,0.724247098f,-0.689540565f,0.580813944f,-0.81403631f,0.416429549f,-0.909168005f,0.237023607f,-0.971503913f,
-0.698376238f,-0.715730846f,0.981379211f,-0.192080393f,0.926210225f,-0.377007425f,0.836547732f,-0.547894061f,0.715730846f,-0.698376238f,0.568258941f,-0.82284981f,
0.399624199f,-0.916679084f,0.216106802f,-0.976369739f,-0.707106769f,-0.707106769f,0.980785251f,-0.195090324f,0.923879504f,-0.382683426f,0.831469595f,-0.555570245f,
0.707106769f,-0.707106769f,0.555570245f,-0.831469595f,0.382683426f,-0.923879504f,0.195090324f,-0.980785251f,-0.715730846f,-0.698376238f,0.980182111f,-0.198098406f,
0.921514034f,-0.388345033f,0.826321065f,-0.563199341f,0.698376238f,-0.715730846f,0.542750776f,-0.839893818f,0.365612984f,-0.93076694f,0.173983872f,-0.984748483f,
-0.724247098f,-0.689540565f,0.979569793f,-0.201104641f,0.919113874f,-0.393992037f,0.8211025f,-0.570780754f,0.689540565f,-0.724247098f,0.529803634f,-0.848120332f,
0.348418683f,-0.937339008f,0.152797192f,-0.988257587f,-0.732654274f,-0.680601001f,0.978948176f,-0.204108968f,0.916679084f,-0.399624199f,0.815814435f,-0.578313768f,
0.680601001f,-0.732654274f,0.516731799f,-0.856147349f,0.331106305f,-0.943593442f,0.13154003f,-0.991310835f,-0.740951121f,-0.671558976f,0.97831738f,-0.207111374f,
0.914209783f,-0.405241311f,0.81045717f,-0.585797846f,0.671558976f,-0.740951121f,0.50353837f,-0.863972843f,0.313681751f,-0.949528158f,0.110222206f,-0.993906975f,
-0.749136388f,-0.662415802f,0.977677345f,-0.210111842f,0.91170603f,-0.410843164f,0.805031359f,-0.593232274f,0.662415802f,-0.749136388f,0.490226477f,-0.871595085f,
0.296150893f,-0.955141187f,0.0888535529f,-0.996044695f,-0.757208824f,-0.653172851f,0.977028131f,-0.213110313f,0.909168005f,-0.416429549f,0.799537241f,-0.600616455f,
0.653172851f,-0.757208824f,0.47679922f,-0.879012227f,0.27851969f,-0.960430503f,0.0674439222f,-0.997723043f,-0.765167236f,-0.643831551f,0.976369739f,-0.216106802f,
0.906595707f,-0.422000259f,0.793975472f,-0.607949793f,0.643831551f,-0.765167236f,0.463259786f,-0.886222541f,0.260794103f,-0.965394437f,0.0460031815f,-0.998941302f,
-0.773010433f,-0.634393275f,0.975702107f,-0.219101235f,0.903989315f,-0.427555084f,0.78834641f,-0.615231574f,0.634393275f,-0.773010433f,0.449611336f,-0.893224299f,
0.242980182f,-0.970031261f,0.024541229f,-0.999698818f,-0.780737221f,-0.624859512f,0.975025356f,-0.222093627f,0.901348829f,-0.433093816f,0.78265059f,-0.622461259f,
0.624859512f,-0.780737221f,0.435857087f,-0.900015891f,0.225083917f,-0.974339366f,0.00306795677f,-0.999995291f,-0.78834641f,-0.615231574f,0.974339366f,-0.225083917f,
0.898674488f,-0.438616246f,0.77688849f,-0.629638255f,0.615231574f,-0.78834641f,0.422000259f,-0.906595707f,0.207111374f,-0.97831738f,-0.0184067301f,-0.999830604f,
-0.795836926f,-0.605511069f,0.973644257f,-0.228072077f,0.895966232f,-0.444122136f,0.771060526f,-0.636761844f,0.605511069f,-0.795836926f,0.408044159f,-0.912962198f,
0.18906866f,-0.981963873f,-0.0398729257f,-0.999204755f,-0.803207517f,-0.59569931f,0.972939968f,-0.231058106f,0.893224299f,-0.449611336f,0.765167236f,-0.643831551f,
0.59569931f,-0.803207517f,0.393992037f,-0.919113874f,0.170961887f,-0.985277653f,-0.061320737f,-0.998118103f,-0.81045717f,-0.585797846f,0.972226501f,-0.234041959f,
0.890448749f,-0.455083579f,0.759209216f,-0.65084666f,0.585797846f,-0.81045717f,0.379847199f,-0.925049245f,0.152797192f,-0.988257587f,-0.0827402622f,-0.996571124f,
-0.817584813f,-0.575808167f,0.971503913f,-0.237023607f,0.887639642f,-0.460538715f,0.753186822f,-0.657806695f,0.575808167f,-0.817584813f,0.365612984f,-0.93076694f,
0.134580702f,-0.990902662f,-0.104121633f,-0.994564593f,-0.824589312f,-0.565731823f,0.970772147f,-0.24000302f,0.884797096f,-0.465976506f,0.747100592f,-0.664710999f,
0.565731823f,-0.824589312f,0.351292759f,-0.936265647f,0.116318628f,-0.993211925f,-0.125454977f,-0.992099285f,-0.831469595f,-0.555570245f,0.970031261f,-0.242980182f,
0.881921291f,-0.471396744f,0.740951121f,-0.671558976f,0.555570245f,-0.831469595f,0.336889863f,-0.941544056f,0.0980171412f,-0.99518472f,-0.146730468f,-0.989176512f,
-0.838224709f,-0.545324981f,0.969281256f,-0.24595505f,0.879012227f,-0.47679922f,0.734738886f,-0.678350031f,0.545324981f,-0.838224709f,0.322407693f,-0.946600914f,
0.0796824396f,-0.996820271f,-0.167938292f,-0.985797524f,-0.84485358f,-0.534997642f,0.968522072f,-0.248927608f,0.876070082f,-0.482183784f,0.728464365f,-0.685083687f,
0.534997642f,-0.84485358f,0.307849646f,-0.95143503f,0.061320737f,-0.998118103f,-0.18906866f,-0.981963873f,-0.851355195f,-0.524589658f,0.967753828f,-0.251897812f,
0.873094976f,-0.487550169f,0.722128212f,-0.691759229f,0.524589658f,-0.851355195f,0.293219149f,-0.95604527f,0.0429382585f,-0.999077737f,-0.210111842f,-0.977677345f,
-0.857728601f,-0.514102757f,0.966976464f,-0.254865646f,0.870086968f,-0.492898196f,0.715730846f,-0.698376238f,0.514102757f,-0.857728601f,0.27851969f,-0.960430503f,
0.024541229f,-0.999698818f,-0.231058106f,-0.972939968f,-0.863972843f,-0.50353837f,0.966189981f,-0.257831097f,0.867046237f,-0.498227656f,0.709272802f,-0.704934061f,
0.50353837f,-0.863972843f,0.263754666f,-0.964589775f,0.00613588467f,-0.999981165f,-0.251897812f,-0.967753828f,-0.870086968f,-0.492898196f,0.965394437f,-0.260794103f,
0.863972843f,-0.50353837f,0.702754736f,-0.711432219f,0.492898196f,-0.870086968f,0.248927608f,-0.968522072f,-0.0122715384f,-0.999924719f,-0.272621363f,-0.962121427f,
-0.876070082f,-0.482183784f,0.964589775f,-0.263754666f,0.860866964f,-0.50883013f,0.696177125f,-0.717870057f,0.482183784f,-0.876070082f,0.234041959f,-0.972226501f,
-0.030674804f,-0.999529421f,-0.293219149f,-0.95604527f,-0.881921291f,-0.471396744f,0.963776052f,-0.266712755f,0.857728601f,-0.514102757f,0.689540565f,-0.724247098f,
0.471396744f,-0.881921291f,0.219101235f,-0.975702107f,-0.0490676761f,-0.99879545f,-0.313681751f,-0.949528158f,-0.887639642f,-0.460538715f,0.962953269f,-0.269668311f,
0.854557991f,-0.519356012f,0.682845533f,-0.730562747f,0.460538715f,-0.887639642f,0.204108968f,-0.978948176f,-0.0674439222f,-0.997723043f,-0.333999664f,-0.94257319f,
-0.893224299f,-0.449611336f,0.962121427f,-0.272621363f,0.851355195f,-0.524589658f,0.676092684f,-0.736816585f,0.449611336f,-0.893224299f,0.18906866f,-0.981963873f,
-0.0857973099f,-0.996312618f,-0.354163527f,-0.935183525f,-0.898674488f,-0.438616246f,0.961280465f,-0.275571823f,0.848120332f,-0.529803634f,0.669282615f,-0.743007958f,
0.438616246f,-0.898674488f,0.173983872f,-0.984748483f,-0.104121633f,-0.994564593f,-0.374164075f,-0.927362502f,-0.903989315f,-0.427555084f,0.960430503f,-0.27851969f,
0.84485358f,-0.534997642f,0.662415802f,-0.749136388f,0.427555084f,-0.903989315f,0.15885815f,-0.987301409f,-0.122410677f,-0.992479563f,-0.393992037f,-0.919113874f,
-0.909168005f,-0.416429549f,0.95957154f,-0.281464934f,0.841554999f,-0.540171444f,0.655492842f,-0.755201399f,0.416429549f,-0.909168005f,0.143695027f,-0.989621997f,
-0.140658244f,-0.990058184f,-0.413638324f,-0.910441279f,-0.914209783f,-0.405241311f,0.958703458f,-0.284407526f,0.838224709f,-0.545324981f,0.64851439f,-0.761202395f,
0.405241311f,-0.914209783f,0.128498107f,-0.991709769f,-0.15885815f,-0.987301409f,-0.433093816f,-0.901348829f,-0.919113874f,-0.393992037f,0.957826436f,-0.287347466f,
0.834862888f,-0.550457954f,0.641481042f,-0.767138898f,0.393992037f,-0.919113874f,0.113270953f,-0.993564129f,-0.177004218f,-0.984210074f,-0.452349573f,-0.891840696f,
-0.923879504f,-0.382683426f,0.956940353f,-0.290284663f,0.831469595f,-0.555570245f,0.634393275f,-0.773010433f,0.382683426f,-0.923879504f,0.0980171412f,-0.99518472f,
-0.195090324f,-0.980785251f,-0.471396744f,-0.881921291f,-0.928506076f,-0.371317208f,0.95604527f,-0.293219149f,0.82804507f,-0.560661554f,0.627251804f,-0.778816521f,
0.371317208f,-0.928506076f,0.0827402622f,-0.996571124f,-0.213110313f,-0.977028131f,-0.490226477f,-0.871595085f,-0.932992816f,-0.359895051f,0.955141187f,-0.296150893f,
0.824589312f,-0.565731823f,0.620057225f,-0.784556568f,0.359895051f,-0.932992816f,0.0674439222f,-0.997723043f,-0.231058106f,-0.972939968f,-0.50883013f,-0.860866964f,
-0.937339008f,-0.348418683f,0.954228103f,-0.299079835f,0.8211025f,-0.570780754f,0.612810075f,-0.790230215f,0.348418683f,-0.937339008f,0.052131705f,-0.998640239f,
-0.248927608f,-0.968522072f,-0.527199149f,-0.849741757f,-0.941544056f,-0.336889863f,0.953306019f,-0.302005947f,0.817584813f,-0.575808167f,0.605511069f,-0.795836926f,
0.336889863f,-0.941544056f,0.0368072242f,-0.999322355f,-0.266712755f,-0.963776052f,-0.545324981f,-0.838224709f,-0.945607305f,-0.32531029f,0.952374995f,-0.304929227f,
0.81403631f,-0.580813944f,0.598160684f,-0.801376164f,0.32531029f,-0.945607305f,0.0214740802f,-0.99976939f,-0.284407526f,-0.958703458f,-0.563199341f,-0.826321065f,
-0.949528158f,-0.313681751f,0.95143503f,-0.307849646f,0.81045717f,-0.585797846f,0.590759695f,-0.806847572f,0.313681751f,-0.949528158f,0.00613588467f,-0.999981165f,
-0.302005947f,-0.953306019f,-0.580813944f,-0.81403631f,-0.953306019f,-0.302005947f,0.950486064f,-0.310767144f,0.806847572f,-0.590759695f,0.583308637f,-0.812250614f,
0.302005947f,-0.953306019f,-0.00920375437f,-0.999957621f,-0.319502026f,-0.947585583f,-0.598160684f,-0.801376164f,-0.956940353f,-0.290284663f,0.949528158f,-0.313681751f,
0.803207517f,-0.59569931f,0.575808167f,-0.817584813f,0.290284663f,-0.956940353f,-0.024541229f,-0.999698818f,-0.336889863f,-0.941544056f,-0.615231574f,-0.78834641f,
-0.960430503f,-0.27851969f,0.94856137f,-0.316593379f,0.799537241f,-0.600616455f,0.568258941f,-0.82284981f,0.27851969f,-0.960430503f,-0.0398729257f,-0.999204755f,
-0.354163527f,-0.935183525f,-0.632018745f,-0.774953127f,-0.963776052f,-0.266712755f,0.947585583f,-0.319502026f,0.795836926f,-0.605511069f,0.560661554f,-0.82804507f,
0.266712755f,-0.963776052f,-0.0551952459f,-0.998475552f,-0.371317208f,-0.928506076f,-0.64851439f,-0.761202395f,-0.966976464f,-0.254865646f,0.946600914f,-0.322407693f,
0.792106569f,-0.610382795f,0.553016722f,-0.833170176f,0.254865646f,-0.966976464f,-0.070504576f,-0.997511446f,-0.388345033f,-0.921514034f,-0.664710999f,-0.747100592f,
-0.970031261f,-0.242980182f,0.945607305f,-0.32531029f,0.78834641f,-0.615231574f,0.545324981f,-0.838224709f,0.242980182f,-0.970031261f,-0.0857973099f,-0.996312618f,
-0.405241311f,-0.914209783f,-0.680601001f,-0.732654274f,-0.972939968f,-0.231058106f,0.944604814f,-0.328209847f,0.784556568f,-0.620057225f,0.537587047f,-0.843208253f,
0.231058106f,-0.972939968f,-0.10106986f,-0.994879305f,-0.422000259f,-0.906595707f,-0.696177125f,-0.717870057f,-0.975702107f,-0.219101235f,0.943593442f,-0.331106305f,
0.780737221f,-0.624859512f,0.529803634f,-0.848120332f,0.219101235f,-0.975702107f,-0.116318628f,-0.993211925f,-0.438616246f,-0.898674488f,-0.711432219f,-0.702754736f,
-0.97831738f,-0.207111374f,0.94257319f,-0.333999664f,0.77688849f,-0.629638255f,0.521975279f,-0.852960587f,0.207111374f,-0.97831738f,-0.13154003f,-0.991310835f,
-0.455083579f,-0.890448749f,-0.726359129f,-0.687315345f,-0.980785251f,-0.195090324f,0.941544056f,-0.336889863f,0.773010433f,-0.634393275f,0.514102757f,-0.857728601f,
0.195090324f,-0.980785251f,-0.146730468f,-0.989176512f,-0.471396744f,-0.881921291f,-0.740951121f,-0.671558976f,-0.983105481f,-0.183039889f,0.940506041f,-0.339776874f,
0.769103348f,-0.639124453f,0.506186664f,-0.862423956f,0.183039889f,-0.983105481f,-0.161886394f,-0.986809373f,-0.487550169f,-0.873094976f,-0.755201399f,-0.655492842f,
-0.985277653f,-0.170961887f,0.939459205f,-0.342660725f,0.765167236f,-0.643831551f,0.498227656f,-0.867046237f,0.170961887f,-0.985277653f,-0.177004218f,-0.984210074f,
-0.50353837f,-0.863972843f,-0.769103348f,-0.639124453f,-0.987301409f,-0.15885815f,0.938403547f,-0.345541328f,0.761202395f,-0.64851439f,0.490226477f,-0.871595085f,
0.15885815f,-0.987301409f,-0.192080393f,-0.981379211f,-0.519356012f,-0.854557991f,-0.78265059f,-0.622461259f,-0.989176512f,-0.146730468f,0.937339008f,-0.348418683f,
0.757208824f,-0.653172851f,0.482183784f,-0.876070082f,0.146730468f,-0.989176512f,-0.207111374f,-0.97831738f,-0.534997642f,-0.84485358f,-0.795836926f,-0.605511069f,
-0.990902662f,-0.134580702f,0.936265647f,-0.351292759f,0.753186822f,-0.657806695f,0.474100202f,-0.880470872f,0.134580702f,-0.990902662f,-0.222093627f,-0.975025356f,
-0.550457954f,-0.834862888f,-0.808656156f,-0.588281572f,-0.992479563f,-0.122410677f,0.935183525f,-0.354163527f,0.749136388f,-0.662415802f,0.465976506f,-0.884797096f,
0.122410677f,-0.992479563f,-0.237023607f,-0.971503913f,-0.565731823f,-0.824589312f,-0.8211025f,-0.570780754f,-0.993906975f,-0.110222206f,0.934092522f,-0.357030958f,
0.745057762f,-0.666999936f,0.457813293f,-0.889048338f,0.110222206f,-0.993906975f,-0.251897812f,-0.967753828f,-0.580813944f,-0.81403631f,-0.833170176f,-0.553016722f,
-0.99518472f,-0.0980171412f,0.932992816f,-0.359895051f,0.740951121f,-0.671558976f,0.449611336f,-0.893224299f,0.0980171412f,-0.99518472f,-0.266712755f,-0.963776052f,
-0.59569931f,-0.803207517f,-0.84485358f,-0.534997642f,-0.996312618f,-0.0857973099f,0.931884289f,-0.362755716f,0.736816585f,-0.676092684f,0.441371262f,-0.897324562f,
0.0857973099f,-0.996312618f,-0.281464934f,-0.95957154f,-0.610382795f,-0.792106569f,-0.856147349f,-0.516731799f,-0.997290432f,-0.0735645667f,0.93076694f,-0.365612984f,
0.732654274f,-0.680601001f,0.433093816f,-0.901348829f,0.0735645667f,-0.997290432f,-0.296150893f,-0.955141187f,-0.624859512f,-0.780737221f,-0.867046237f,-0.498227656f,
-0.998118103f,-0.061320737f,0.929640889f,-0.368466824f,0.728464365f,-0.685083687f,0.424779683f,-0.905296743f,0.061320737f,-0.998118103f,-0.310767144f,-0.950486064f,
-0.639124453f,-0.769103348f,-0.877545297f,-0.479493767f,-0.99879545f,-0.0490676761f,0.928506076f,-0.371317208f,0.724247098f,-0.689540565f,0.416429549f,-0.909168005f,
0.0490676761f,-0.99879545f,-0.32531029f,-0.945607305f,-0.653172851f,-0.757208824f,-0.887639642f,-0.460538715f,-0.999322355f,-0.0368072242f,0.927362502f,-0.374164075f,
0.720002532f,-0.693971455f,0.408044159f,-0.912962198f,0.0368072242f,-0.999322355f,-0.339776874f,-0.940506041f,-0.666999936f,-0.745057762f,-0.897324562f,-0.441371262f,
-0.999698818f,-0.024541229f,0.926210225f,-0.377007425f,0.715730846f,-0.698376238f,0.399624199f,-0.916679084f,0.024541229f,-0.999698818f,-0.354163527f,-0.935183525f,
-0.680601001f,-0.732654274f,-0.906595707f,-0.422000259f,-0.999924719f,-0.0122715384f,0.925049245f,-0.379847199f,0.711432219f,-0.702754736f,0.391170382f,-0.920318305f,
0.0122715384f,-0.999924719f,-0.368466824f,-0.929640889f,-0.693971455f,-0.720002532f,-0.915448725f,-0.402434647f,-1.0f,0.0f,0.923879504f,-0.382683426f,
0.707106769f,-0.707106769f,0.382683426f,-0.923879504f,0.0f,-1.0f,-0.382683426f,-0.923879504f,-0.707106769f,-0.707106769f,-0.923879504f,-0.382683426f,
-0.999924719f,0.0122715384f,0.92270112f,-0.385516047f,0.702754736f,-0.711432219f,0.374164075f,-0.927362502f,-0.0122715384f,-0.999924719f,-0.396809995f,-0.917900801f,
-0.720002532f,-0.693971455f,-0.931884289f,-0.362755716f,-0.999698818f,0.024541229f,0.921514034f,-0.388345033f,0.698376238f,-0.715730846f,0.365612984f,-0.93076694f,
-0.024541229f,-0.999698818f,-0.410843164f,-0.91170603f,-0.732654274f,-0.680601001f,-0.939459205f,-0.342660725f,-0.999322355f,0.0368072242f,0.920318305f,-0.391170382f,
0.693971455f,-0.720002532f,0.357030958f,-0.934092522f,-0.0368072242f,-0.999322355f,-0.424779683f,-0.905296743f,-0.745057762f,-0.666999936f,-0.946600914f,-0.322407693f,
-0.99879545f,0.0490676761f,0.919113874f,-0.393992037f,0.689540565f,-0.724247098f,0.348418683f,-0.937339008f,-0.0490676761f,-0.99879545f,-0.438616246f,-0.898674488f,
-0.757208824f,-0.653172851f,-0.953306019f,-0.302005947f,-0.998118103f,0.061320737f,0.917900801f,-0.396809995f,0.685083687f,-0.728464365f,0.339776874f,-0.940506041f,
-0.061320737f,-0.998118103f,-0.452349573f,-0.891840696f,-0.769103348f,-0.639124453f,-0.95957154f,-0.281464934f,-0.997290432f,0.0735645667f,0.916679084f,-0.399624199f,
0.680601001f,-0.732654274f,0.331106305f,-0.943593442f,-0.0735645667f,-0.997290432f,-0.465976506f,-0.884797096f,-0.780737221f,-0.624859512f,-0.965394437f,-0.260794103f,
-0.996312618f,0.0857973099f,0.915448725f,-0.402434647f,0.676092684f,-0.736816585f,0.322407693f,-0.946600914f,-0.0857973099f,-0.996312618f,-0.479493767f,-0.877545297f,
-0.792106569f,-0.610382795f,-0.970772147f,-0.24000302f,-0.99518472f,0.0980171412f,0.914209783f,-0.405241311f,0.671558976f,-0.740951121f,0.313681751f,-0.949528158f,
-0.0980171412f,-0.99518472f,-0.492898196f,-0.870086968f,-0.803207517f,-0.59569931f,-0.975702107f,-0.219101235f,-0.993906975f,0.110222206f,0.912962198f,-0.408044159f,
0.666999936f,-0.745057762f,0.304929227f,-0.952374995f,-0.110222206f,-0.993906975f,-0.506186664f,-0.862423956f,-0.81403631f,-0.580813944f,-0.980182111f,-0.198098406f,
-0.992479563f,0.122410677f,0.91170603f,-0.410843164f,0.662415802f,-0.749136388f,0.296150893f,-0.955141187f,-0.122410677f,-0.992479563f,-0.519356012f,-0.854557991f,
-0.824589312f,-0.565731823f,-0.984210074f,-0.177004218f,-0.990902662f,0.134580702f,0.910441279f,-0.413638324f,0.657806695f,-0.753186822f,0.287347466f,-0.957826436f,
-0.134580702f,-0.990902662f,-0.532403111f,-0.84649092f,-0.834862888f,-0.550457954f,-0.987784147f,-0.155828401f,-0.989176512f,0.146730468f,0.909168005f,-0.416429549f,
0.653172851f,-0.757208824f,0.27851969f,-0.960430503f,-0.146730468f,-0.989176512f,-0.545324981f,-0.838224709f,-0.84485358f,-0.534997642f,-0.990902662f,-0.134580702f,
-0.987301409f,0.15885815f,0.907886088f,-0.419216901f,0.64851439f,-0.761202395f,0.269668311f,-0.962953269f,-0.15885815f,-0.987301409f,-0.558118522f,-0.829761207f,
-0.854557991f,-0.519356012f,-0.993564129f,-0.113270953f,-0.985277653f,0.170961887f,0.906595707f,-0.422000259f,0.643831551f,-0.765167236f,0.260794103f,-0.965394437f,
-0.170961887f,-0.985277653f,-0.570780754f,-0.8211025f,-0.863972843f,-0.50353837f,-0.995767415f,-0.0919089541f,-0.983105481f,0.183039889f,0.905296743f,-0.424779683f,
0.639124453f,-0.769103348f,0.251897812f,-0.967753828f,-0.183039889f,-0.983105481f,-0.583308637f,-0.812250614f,-0.873094976f,-0.487550169f,-0.997511446f,-0.070504576f,
-0.980785251f,0.195090324f,0.903989315f,-0.427555084f,0.634393275f,-0.773010433f,0.242980182f,-0.970031261f,-0.195090324f,-0.980785251f,-0.59569931f,-0.803207517f,
-0.881921291f,-0.471396744f,-0.99879545f,-0.0490676761f,-0.97831738f,0.207111374f,0.902673304f,-0.430326492f,0.629638255f,-0.77688849f,0.234041959f,-0.972226501f,
-0.207111374f,-0.97831738f,-0.607949793f,-0.793975472f,-0.890448749f,-0.455083579f,-0.999618828f,-0.027608145f,-0.975702107f,0.219101235f,0.901348829f,-0.433093816f,
0.624859512f,-0.780737221f,0.225083917f,-0.974339366f,-0.219101235f,-0.975702107f,-0.620057225f,-0.784556568f,-0.898674488f,-0.438616246f,-0.999981165f,-0.00613588467f,
-0.972939968f,0.231058106f,0.900015891f,-0.435857087f,0.620057225f,-0.784556568f,0.216106802f,-0.976369739f,-0.231058106f,-0.972939968f,-0.632018745f,-0.774953127f,
-0.906595707f,-0.422000259f,-0.99988234f,0.015339206f,-0.970031261f,0.242980182f,0.898674488f,-0.438616246f,0.615231574f,-0.78834641f,0.207111374f,-0.97831738f,
-0.242980182f,-0.970031261f,-0.643831551f,-0.765167236f,-0.914209783f,-0.405241311f,-0.999322355f,0.0368072242f,-0.966976464f,0.254865646f,0.897324562f,-0.441371262f,
0.610382795f,-0.792106569f,0.198098406f,-0.980182111f,-0.254865646f,-0.966976464f,-0.655492842f,-0.755201399f,-0.921514034f,-0.388345033f,-0.998301566f,0.0582582653f,
-0.963776052f,0.266712755f,0.895966232f,-0.444122136f,0.605511069f,-0.795836926f,0.18906866f,-0.981963873f,-0.266712755f,-0.963776052f,-0.666999936f,-0.745057762f,
-0.928506076f,-0.371317208f,-0.996820271f,0.0796824396f,-0.960430503f,0.27851969f,0.894599497f,-0.446868837f,0.600616455f,-0.799537241f,0.180022895f,-0.983662426f,
-0.27851969f,-0.960430503f,-0.678350031f,-0.734738886f,-0.935183525f,-0.354163527f,-0.994879305f,0.10106986f,-0.956940353f,0.290284663f,0.893224299f,-0.449611336f,
0.59569931f,-0.803207517f,0.170961887f,-0.985277653f,-0.290284663f,-0.956940353f,-0.689540565f,-0.724247098f,-0.941544056f,-0.336889863f,-0.992479563f,0.122410677f,
-0.953306019f,0.302005947f,0.891840696f,-0.452349573f,0.590759695f,-0.806847572f,0.161886394f,-0.986809373f,-0.302005947f,-0.953306019f,-0.700568795f,-0.71358484f,
-0.947585583f,-0.319502026f,-0.989621997f,0.143695027f,-0.949528158f,0.313681751f,0.890448749f,-0.455083579f,0.585797846f,-0.81045717f,0.152797192f,-0.988257587f,
-0.313681751f,-0.949528158f,-0.711432219f,-0.702754736f,-0.953306019f,-0.302005947f,-0.986308098f,0.164913118f,-0.945607305f,0.32531029f,0.889048338f,-0.457813293f,
0.580813944f,-0.81403631f,0.143695027f,-0.989621997f,-0.32531029f,-0.945607305f,-0.722128212f,-0.691759229f,-0.958703458f,-0.284407526f,-0.982539296f,0.186055154f,
-0.941544056f,0.336889863f,0.887639642f,-0.460538715f,0.575808167f,-0.817584813f,0.134580702f,-0.990902662f,-0.336889863f,-0.941544056f,-0.732654274f,-0.680601001f,
-0.963776052f,-0.266712755f,-0.97831738f,0.207111374f,-0.937339008f,0.348418683f,0.886222541f,-0.463259786f,0.570780754f,-0.8211025f,0.125454977f,-0.992099285f,
-0.348418683f,-0.937339008f,-0.743007958f,-0.669282615f,-0.968522072f,-0.248927608f,-0.973644257f,0.228072077f,-0.932992816f,0.359895051f,0.884797096f,-0.465976506f,
0.565731823f,-0.824589312f,0.116318628f,-0.993211925f,-0.359895051f,-0.932992816f,-0.753186822f,-0.657806695f,-0.972939968f,-0.231058106f,-0.968522072f,0.248927608f,
-0.928506076f,0.371317208f,0.883363366f,-0.468688816f,0.560661554f,-0.82804507f,0.107172422f,-0.994240463f,-0.371317208f,-0.928506076f,-0.763188422f,-0.64617604f,
-0.977028131f,-0.213110313f,-0.962953269f,0.269668311f,-0.923879504f,0.382683426f,0.881921291f,-0.471396744f,0.555570245f,-0.831469595f,0.0980171412f,-0.99518472f,
-0.382683426f,-0.923879504f,-0.773010433f,-0.634393275f,-0.980785251f,-0.195090324f,-0.956940353f,0.290284663f,-0.919113874f,0.393992037f,0.880470872f,-0.474100202f,
0.550457954f,-0.834862888f,0.0888535529f,-0.996044695f,-0.393992037f,-0.919113874f,-0.78265059f,-0.622461259f,-0.984210074f,-0.177004218f,-0.950486064f,0.310767144f,
-0.914209783f,0.405241311f,0.879012227f,-0.47679922f,0.545324981f,-0.838224709f,0.0796824396f,-0.996820271f,-0.405241311f,-0.914209783f,-0.792106569f,-0.610382795f,
-0.987301409f,-0.15885815f,-0.943593442f,0.331106305f,-0.909168005f,0.416429549f,0.877545297f,-0.479493767f,0.540171444f,-0.841554999f,0.070504576f,-0.997511446f,
-0.416429549f,-0.909168005f,-0.801376164f,-0.598160684f,-0.990058184f,-0.140658244f,-0.936265647f,0.351292759f,-0.903989315f,0.427555084f,0.876070082f,-0.482183784f,
0.534997642f,-0.84485358f,0.061320737f,-0.998118103f,-0.427555084f,-0.903989315f,-0.81045717f,-0.585797846f,-0.992479563f,-0.122410677f,-0.928506076f,0.371317208f,
-0.898674488f,0.438616246f,0.874586642f,-0.484869242f,0.529803634f,-0.848120332f,0.052131705f,-0.998640239f,-0.438616246f,-0.898674488f,-0.819347501f,-0.573297143f,
-0.994564593f,-0.104121633f,-0.920318305f,0.391170382f,-0.893224299f,0.449611336f,0.873094976f,-0.487550169f,0.524589658f,-0.851355195f,0.0429382585f,-0.999077737f,
-0.449611336f,-0.893224299f,-0.82804507f,-0.560661554f,-0.996312618f,-0.0857973099f,-0.91170603f,0.410843164f,-0.887639642f,0.460538715f,0.871595085f,-0.490226477f,
0.519356012f,-0.854557991f,0.0337411724f,-0.999430597f,-0.460538715f,-0.887639642f,-0.836547732f,-0.547894061f,-0.997723043f,-0.0674439222f,-0.902673304f,0.430326492f,
-0.881921291f,0.471396744f,0.870086968f,-0.492898196f,0.514102757f,-0.857728601f,0.024541229f,-0.999698818f,-0.471396744f,-0.881921291f,-0.84485358f,-0.534997642f,
-0.99879545f,-0.0490676761f,-0.893224299f,0.449611336f,-0.876070082f,0.482183784f,0.868570685f,-0.495565265f,0.50883013f,-0.860866964f,0.015339206f,-0.99988234f,
-0.482183784f,-0.876070082f,-0.852960587f,-0.521975279f,-0.999529421f,-0.030674804f,-0.883363366f,0.468688816f,-0.870086968f,0.492898196f,0.867046237f,-0.498227656f,
0.50353837f,-0.863972843f,0.00613588467f,-0.999981165f,-0.492898196f,-0.870086968f,-0.860866964f,-0.50883013f,-0.999924719f,-0.0122715384f,-0.873094976f,0.487550169f,
-0.863972843f,0.50353837f,0.865513623f,-0.500885367f,0.498227656f,-0.867046237f,-0.00306795677f,-0.999995291f,-0.50353837f,-0.863972843f,-0.868570685f,-0.495565265f,
-0.999981165f,0.00613588467f,-0.862423956f,0.506186664f,-0.857728601f,0.514102757f,0.863972843f,-0.50353837f,0.492898196f,-0.870086968f,-0.0122715384f,-0.999924719f,
-0.514102757f,-0.857728601f,-0.876070082f,-0.482183784f,-0.999698818f,0.024541229f,-0.851355195f,0.524589658f,-0.851355195f,0.524589658f,0.862423956f,-0.506186664f,
0.487550169f,-0.873094976f,-0.0214740802f,-0.99976939f,-0.524589658f,-0.851355195f,-0.883363366f,-0.468688816f,-0.999077737f,0.0429382585f,-0.839893818f,0.542750776f,
-0.84485358f,0.534997642f,0.860866964f,-0.50883013f,0.482183784f,-0.876070082f,-0.030674804f,-0.999529421f,-0.534997642f,-0.84485358f,-0.890448749f,-0.455083579f,
-0.998118103f,0.061320737f,-0.82804507f,0.560661554f,-0.838224709f,0.545324981f,0.859301805f,-0.511468828f,0.47679922f,-0.879012227f,-0.0398729257f,-0.999204755f,
-0.545324981f,-0.838224709f,-0.897324562f,-0.441371262f,-0.996820271f,0.0796824396f,-0.815814435f,0.578313768f,-0.831469595f,0.555570245f,0.857728601f,-0.514102757f,
0.471396744f,-0.881921291f,-0.0490676761f,-0.99879545f,-0.555570245f,-0.831469595f,-0.903989315f,-0.427555084f,-0.99518472f,0.0980171412f,-0.803207517f,0.59569931f,
-0.824589312f,0.565731823f,0.856147349f,-0.516731799f,0.465976506f,-0.884797096f,-0.0582582653f,-0.998301566f,-0.565731823f,-0.824589312f,-0.910441279f,-0.413638324f,
-0.993211925f,0.116318628f,-0.790230215f,0.612810075f,-0.817584813f,0.575808167f,0.854557991f,-0.519356012f,0.460538715f,-0.887639642f,-0.0674439222f,-0.997723043f,
-0.575808167f,-0.817584813f,-0.916679084f,-0.399624199f,-0.990902662f,0.134580702f,-0.77688849f,0.629638255f,-0.81045717f,0.585797846f,0.852960587f,-0.521975279f,
0.455083579f,-0.890448749f,-0.0766238645f,-0.997060061f,-0.585797846f,-0.81045717f,-0.92270112f,-0.385516047f,-0.988257587f,0.152797192f,-0.763188422f,0.64617604f,
-0.803207517f,0.59569931f,0.851355195f,-0.524589658f,0.449611336f,-0.893224299f,-0.0857973099f,-0.996312618f,-0.59569931f,-0.803207517f,-0.928506076f,-0.371317208f,
-0.985277653f,0.170961887f,-0.749136388f,0.662415802f,-0.795836926f,0.605511069f,0.849741757f,-0.527199149f,0.444122136f,-0.895966232f,-0.0949634984f,-0.995480776f,
-0.605511069f,-0.795836926f,-0.934092522f,-0.357030958f,-0.981963873f,0.18906866f,-0.734738886f,0.678350031f,-0.78834641f,0.615231574f,0.848120332f,-0.529803634f,
0.438616246f,-0.898674488f,-0.104121633f,-0.994564593f,-0.615231574f,-0.78834641f,-0.939459205f,-0.342660725f,-0.97831738f,0.207111374f,-0.720002532f,0.693971455f,
-0.780737221f,0.624859512f,0.84649092f,-0.532403111f,0.433093816f,-0.901348829f,-0.113270953f,-0.993564129f,-0.624859512f,-0.780737221f,-0.944604814f,-0.328209847f,
-0.974339366f,0.225083917f,-0.704934061f,0.709272802f,-0.773010433f,0.634393275f,0.84485358f,-0.534997642f,0.427555084f,-0.903989315f,-0.122410677f,-0.992479563f,
-0.634393275f,-0.773010433f,-0.949528158f,-0.313681751f,-0.970031261f,0.242980182f,-0.689540565f,0.724247098f,-0.765167236f,0.643831551f,0.843208253f,-0.537587047f,
0.422000259f,-0.906595707f,-0.13154003f,-0.991310835f,-0.643831551f,-0.765167236f,-0.954228103f,-0.299079835f,-0.965394437f,0.260794103f,-0.673829019f,0.73888731f,
-0.757208824f,0.653172851f,0.841554999f,-0.540171444f,0.416429549f,-0.909168005f,-0.140658244f,-0.990058184f,-0.653172851f,-0.757208824f,-0.958703458f,-0.284407526f,
-0.960430503f,0.27851969f,-0.657806695f,0.753186822f,-0.749136388f,0.662415802f,0.839893818f,-0.542750776f,0.410843164f,-0.91170603f,-0.149764538f,-0.988721669f,
-0.662415802f,-0.749136388f,-0.962953269f,-0.269668311f,-0.955141187f,0.296150893f,-0.641481042f,0.767138898f,-0.740951121f,0.671558976f,0.838224709f,-0.545324981f,
0.405241311f,-0.914209783f,-0.15885815f,-0.987301409f,-0.671558976f,-0.740951121f,-0.966976464f,-0.254865646f,-0.949528158f,0.313681751f,-0.624859512f,0.780737221f,
-0.732654274f,0.680601001f,0.836547732f,-0.547894061f,0.399624199f,-0.916679084f,-0.167938292f,-0.985797524f,-0.680601001f,-0.732654274f,-0.970772147f,-0.24000302f,
-0.943593442f,0.331106305f,-0.607949793f,0.793975472f,-0.724247098f,0.689540565f,0.834862888f,-0.550457954f,0.393992037f,-0.919113874f,-0.177004218f,-0.984210074f,
-0.689540565f,-0.724247098f,-0.974339366f,-0.225083917f,-0.937339008f,0.348418683f,-0.590759695f,0.806847572f,-0.715730846f,0.698376238f,0.833170176f,-0.553016722f,
0.388345033f,-0.921514034f,-0.186055154f,-0.982539296f,-0.698376238f,-0.715730846f,-0.977677345f,-0.210111842f,-0.93076694f,0.365612984f,-0.573297143f,0.819347501f,
-0.707106769f,0.707106769f,0.831469595f,-0.555570245f,0.382683426f,-0.923879504f,-0.195090324f,-0.980785251f,-0.707106769f,-0.707106769f,-0.980785251f,-0.195090324f,
-0.923879504f,0.382683426f,-0.555570245f,0.831469595f,-0.698376238f,0.715730846f,0.829761207f,-0.558118522f,0.377007425f,-0.926210225f,-0.204108968f,-0.978948176f,
-0.715730846f,-0.698376238f,-0.983662426f,-0.180022895f,-0.916679084f,0.399624199f,-0.537587047f,0.843208253f,-0.689540565f,0.724247098f,0.82804507f,-0.560661554f,
0.371317208f,-0.928506076f,-0.213110313f,-0.977028131f,-0.724247098f,-0.689540565f,-0.986308098f,-0.164913118f,-0.909168005f,0.416429549f,-0.519356012f,0.854557991f,
-0.680601001f,0.732654274f,0.826321065f,-0.563199341f,0.365612984f,-0.93076694f,-0.222093627f,-0.975025356f,-0.732654274f,-0.680601001f,-0.988721669f,-0.149764538f,
-0.901348829f,0.433093816f,-0.500885367f,0.865513623f,-0.671558976f,0.740951121f,0.824589312f,-0.565731823f,0.359895051f,-0.932992816f,-0.231058106f,-0.972939968f,
-0.740951121f,-0.671558976f,-0.990902662f,-0.134580702f,-0.893224299f,0.449611336f,-0.482183784f,0.876070082f,-0.662415802f,0.749136388f,0.82284981f,-0.568258941f,
0.354163527f,-0.935183525f,-0.24000302f,-0.970772147f,-0.749136388f,-0.662415802f,-0.992850423f,-0.119365215f,-0.884797096f,0.465976506f,-0.463259786f,0.886222541f,
-0.653172851f,0.757208824f,0.8211025f,-0.570780754f,0.348418683f,-0.937339008f,-0.248927608f,-0.968522072f,-0.757208824f,-0.653172851f,-0.994564593f,-0.104121633f,
-0.876070082f,0.482183784f,-0.444122136f,0.895966232f,-0.643831551f,0.765167236f,0.819347501f,-0.573297143f,0.342660725f,-0.939459205f,-0.257831097f,-0.966189981f,
-0.765167236f,-0.643831551f,-0.996044695f,-0.0888535529f,-0.867046237f,0.498227656f,-0.424779683f,0.905296743f,-0.634393275f,0.773010433f,0.817584813f,-0.575808167f,
0.336889863f,-0.941544056f,-0.266712755f,-0.963776052f,-0.773010433f,-0.634393275f,-0.997290432f,-0.0735645667f,-0.857728601f,0.514102757f,-0.405241311f,0.914209783f,
-0.624859512f,0.780737221f,0.815814435f,-0.578313768f,0.331106305f,-0.943593442f,-0.275571823f,-0.961280465f,-0.780737221f,-0.624859512f,-0.998301566f,-0.0582582653f,
-0.848120332f,0.529803634f,-0.385516047f,0.92270112f,-0.615231574f,0.78834641f,0.81403631f,-0.580813944f,0.32531029f,-0.945607305f,-0.284407526f,-0.958703458f,
-0.78834641f,-0.615231574f,-0.999077737f,-0.0429382585f,-0.838224709f,0.545324981f,-0.365612984f,0.93076694f,-0.605511069f,0.795836926f,0.812250614f,-0.583308637f,
0.319502026f,-0.947585583f,-0.293219149f,-0.95604527f,-0.795836926f,-0.605511069f,-0.999618828f,-0.027608145f,-0.82804507f,0.560661554f,-0.345541328f,0.938403547f,
-0.59569931f,0.803207517f,0.81045717f,-0.585797846f,0.313681751f,-0.949528158f,-0.302005947f,-0.953306019f,-0.803207517f,-0.59569931f,-0.999924719f,-0.0122715384f,
-0.817584813f,0.575808167f,-0.32531029f,0.945607305f,-0.585797846f,0.81045717f,0.808656156f,-0.588281572f,0.307849646f,-0.95143503f,-0.310767144f,-0.950486064f,
-0.81045717f,-0.585797846f,-0.999995291f,0.00306795677f,-0.806847572f,0.590759695f,-0.304929227f,0.952374995f,-0.575808167f,0.817584813f,0.806847572f,-0.590759695f,
0.302005947f,-0.953306019f,-0.319502026f,-0.947585583f,-0.817584813f,-0.575808167f,-0.999830604f,0.0184067301f,-0.795836926f,0.605511069f,-0.284407526f,0.958703458f,
-0.565731823f,0.824589312f,0.805031359f,-0.593232274f,0.296150893f,-0.955141187f,-0.328209847f,-0.944604814f,-0.824589312f,-0.565731823f,-0.999430597f,0.0337411724f,
-0.784556568f,0.620057225f,-0.263754666f,0.964589775f,-0.555570245f,0.831469595f,0.803207517f,-0.59569931f,0.290284663f,-0.956940353f,-0.336889863f,-0.941544056f,
-0.831469595f,-0.555570245f,-0.99879545f,0.0490676761f,-0.773010433f,0.634393275f,-0.242980182f,0.970031261f,-0.545324981f,0.838224709f,0.801376164f,-0.598160684f,
0.284407526f,-0.958703458f,-0.345541328f,-0.938403547f,-0.838224709f,-0.545324981f,-0.997925282f,0.0643826276f,-0.761202395f,0.64851439f,-0.222093627f,0.975025356f,
-0.534997642f,0.84485358f,0.799537241f,-0.600616455f,0.27851969f,-0.960430503f,-0.354163527f,-0.935183525f,-0.84485358f,-0.534997642f,-0.996820271f,0.0796824396f,
-0.749136388f,0.662415802f,-0.201104641f,0.979569793f,-0.524589658f,0.851355195f,0.797690868f,-0.603066623f,0.272621363f,-0.962121427f,-0.362755716f,-0.931884289f,
-0.851355195f,-0.524589658f,-0.995480776f,0.0949634984f,-0.736816585f,0.676092684f,-0.180022895f,0.983662426f,-0.514102757f,0.857728601f,0.795836926f,-0.605511069f,
0.266712755f,-0.963776052f,-0.371317208f,-0.928506076f,-0.857728601f,-0.514102757f,-0.993906975f,0.110222206f,-0.724247098f,0.689540565f,-0.15885815f,0.987301409f,
-0.50353837f,0.863972843f,0.793975472f,-0.607949793f,0.260794103f,-0.965394437f,-0.379847199f,-0.925049245f,-0.863972843f,-0.50353837f,-0.992099285f,0.125454977f,
-0.711432219f,0.702754736f,-0.137620121f,0.990485072f,-0.492898196f,0.870086968f,0.792106569f,-0.610382795f,0.254865646f,-0.966976464f,-0.388345033f,-0.921514034f,
-0.870086968f,-0.492898196f,-0.990058184f,0.140658244f,-0.698376238f,0.715730846f,-0.116318628f,0.993211925f,-0.482183784f,0.876070082f,0.790230215f,-0.612810075f,
0.248927608f,-0.968522072f,-0.396809995f,-0.917900801f,-0.876070082f,-0.482183784f,-0.987784147f,0.155828401f,-0.685083687f,0.728464365f,-0.0949634984f,0.995480776f,
-0.471396744f,0.881921291f,0.78834641f,-0.615231574f,0.242980182f,-0.970031261f,-0.405241311f,-0.914209783f,-0.881921291f,-0.471396744f,-0.985277653f,0.170961887f,
-0.671558976f,0.740951121f,-0.0735645667f,0.997290432f,-0.460538715f,0.887639642f,0.786455214f,-0.61764729f,0.237023607f,-0.971503913f,-0.413638324f,-0.910441279f,
-0.887639642f,-0.460538715f,-0.982539296f,0.186055154f,-0.657806695f,0.753186822f,-0.052131705f,0.998640239f,-0.449611336f,0.893224299f,0.784556568f,-0.620057225f,
0.231058106f,-0.972939968f,-0.422000259f,-0.906595707f,-0.893224299f,-0.449611336f,-0.979569793f,0.201104641f,-0.643831551f,0.765167236f,-0.030674804f,0.999529421f,
-0.438616246f,0.898674488f,0.78265059f,-0.622461259f,0.225083917f,-0.974339366f,-0.430326492f,-0.902673304f,-0.898674488f,-0.438616246f,-0.976369739f,0.216106802f,
-0.629638255f,0.77688849f,-0.00920375437f,0.999957621f,-0.427555084f,0.903989315f,0.780737221f,-0.624859512f,0.219101235f,-0.975702107f,-0.438616246f,-0.898674488f,
-0.903989315f,-0.427555084f,-0.972939968f,0.231058106f,-0.615231574f,0.78834641f,0.0122715384f,0.999924719f,-0.416429549f,0.909168005f,0.778816521f,-0.627251804f,
0.213110313f,-0.977028131f,-0.446868837f,-0.894599497f,-0.909168005f,-0.416429549f,-0.969281256f,0.24595505f,-0.600616455f,0.799537241f,0.0337411724f,0.999430597f,
-0.405241311f,0.914209783f,0.77688849f,-0.629638255f,0.207111374f,-0.97831738f,-0.455083579f,-0.890448749f,-0.914209783f,-0.405241311f,-0.965394437f,0.260794103f,
-0.585797846f,0.81045717f,0.0551952459f,0.998475552f,-0.393992037f,0.919113874f,0.774953127f,-0.632018745f,0.201104641f,-0.979569793f,-0.463259786f,-0.886222541f,
-0.919113874f,-0.393992037f,-0.961280465f,0.275571823f,-0.570780754f,0.8211025f,0.0766238645f,0.997060061f,-0.382683426f,0.923879504f,0.773010433f,-0.634393275f,
0.195090324f,-0.980785251f,-0.471396744f,-0.881921291f,-0.923879504f,-0.382683426f,-0.956940353f,0.290284663f,-0.555570245f,0.831469595f,0.0980171412f,0.99518472f,
-0.371317208f,0.928506076f,0.771060526f,-0.636761844f,0.18906866f,-0.981963873f,-0.479493767f,-0.877545297f,-0.928506076f,-0.371317208f,-0.952374995f,0.304929227f,
-0.540171444f,0.841554999f,0.119365215f,0.992850423f,-0.359895051f,0.932992816f,0.769103348f,-0.639124453f,0.183039889f,-0.983105481f,-0.487550169f,-0.873094976f,
-0.932992816f,-0.359895051f,-0.947585583f,0.319502026f,-0.524589658f,0.851355195f,0.140658244f,0.990058184f,-0.348418683f,0.937339008f,0.767138898f,-0.641481042f,
0.177004218f,-0.984210074f,-0.495565265f,-0.868570685f,-0.937339008f,-0.348418683f,-0.94257319f,0.333999664f,-0.50883013f,0.860866964f,0.161886394f,0.986809373f,
-0.336889863f,0.941544056f,0.765167236f,-0.643831551f,0.170961887f,-0.985277653f,-0.50353837f,-0.863972843f,-0.941544056f,-0.336889863f,-0.937339008f,0.348418683f,
-0.492898196f,0.870086968f,0.183039889f,0.983105481f,-0.32531029f,0.945607305f,0.763188422f,-0.64617604f,0.164913118f,-0.986308098f,-0.511468828f,-0.859301805f,
-0.945607305f,-0.32531029f,-0.931884289f,0.362755716f,-0.47679922f,0.879012227f,0.204108968f,0.978948176f,-0.313681751f,0.949528158f,0.761202395f,-0.64851439f,
0.15885815f,-0.987301409f,-0.519356012f,-0.854557991f,-0.949528158f,-0.313681751f,-0.926210225f,0.377007425f,-0.460538715f,0.887639642f,0.225083917f,0.974339366f,
-0.302005947f,0.953306019f,0.759209216f,-0.65084666f,0.152797192f,-0.988257587f,-0.527199149f,-0.849741757f,-0.953306019f,-0.302005947f,-0.920318305f,0.391170382f,
-0.444122136f,0.895966232f,0.24595505f,0.969281256f,-0.290284663f,0.956940353f,0.757208824f,-0.653172851f,0.146730468f,-0.989176512f,-0.534997642f,-0.84485358f,
-0.956940353f,-0.290284663f,-0.914209783f,0.405241311f,-0.427555084f,0.903989315f,0.266712755f,0.963776052f,-0.27851969f,0.960430503f,0.755201399f,-0.655492842f,
0.140658244f,-0.990058184f,-0.542750776f,-0.839893818f,-0.960430503f,-0.27851969f,-0.907886088f,0.419216901f,-0.410843164f,0.91170603f,0.287347466f,0.957826436f,
-0.266712755f,0.963776052f,0.753186822f,-0.657806695f,0.134580702f,-0.990902662f,-0.550457954f,-0.834862888f,-0.963776052f,-0.266712755f,-0.901348829f,0.433093816f,
-0.393992037f,0.919113874f,0.307849646f,0.95143503f,-0.254865646f,0.966976464f,0.751165152f,-0.660114348f,0.128498107f,-0.991709769f,-0.558118522f,-0.829761207f,
-0.966976464f,-0.254865646f,-0.894599497f,0.446868837f,-0.377007425f,0.926210225f,0.328209847f,0.944604814f,-0.242980182f,0.970031261f,0.749136388f,-0.662415802f,
0.122410677f,-0.992479563f,-0.565731823f,-0.824589312f,-0.970031261f,-0.242980182f,-0.887639642f,0.460538715f,-0.359895051f,0.932992816f,0.348418683f,0.937339008f,
-0.231058106f,0.972939968f,0.747100592f,-0.664710999f,0.116318628f,-0.993211925f,-0.573297143f,-0.819347501f,-0.972939968f,-0.231058106f,-0.880470872f,0.474100202f,
-0.342660725f,0.939459205f,0.368466824f,0.929640889f,-0.219101235f,0.975702107f,0.745057762f,-0.666999936f,0.110222206f,-0.993906975f,-0.580813944f,-0.81403631f,
-0.975702107f,-0.219101235f,-0.873094976f,0.487550169f,-0.32531029f,0.945607305f,0.388345033f,0.921514034f,-0.207111374f,0.97831738f,0.743007958f,-0.669282615f,
0.104121633f,-0.994564593f,-0.588281572f,-0.808656156f,-0.97831738f,-0.207111374f,-0.865513623f,0.500885367f,-0.307849646f,0.95143503f,0.408044159f,0.912962198f,
-0.195090324f,0.980785251f,0.740951121f,-0.671558976f,0.0980171412f,-0.99518472f,-0.59569931f,-0.803207517f,-0.980785251f,-0.195090324f,-0.857728601f,0.514102757f,
-0.290284663f,0.956940353f,0.427555084f,0.903989315f,-0.183039889f,0.983105481f,0.73888731f,-0.673829019f,0.0919089541f,-0.995767415f,-0.603066623f,-0.797690868f,
-0.983105481f,-0.183039889f,-0.849741757f,0.527199149f,-0.272621363f,0.962121427f,0.446868837f,0.894599497f,-0.170961887f,0.985277653f,0.736816585f,-0.676092684f,
0.0857973099f,-0.996312618f,-0.610382795f,-0.792106569f,-0.985277653f,-0.170961887f,-0.841554999f,0.540171444f,-0.254865646f,0.966976464f,0.465976506f,0.884797096f,
-0.15885815f,0.987301409f,0.734738886f,-0.678350031f,0.0796824396f,-0.996820271f,-0.61764729f,-0.786455214f,-0.987301409f,-0.15885815f,-0.833170176f,0.553016722f,
-0.237023607f,0.971503913f,0.484869242f,0.874586642f,-0.146730468f,0.989176512f,0.732654274f,-0.680601001f,0.0735645667f,-0.997290432f,-0.624859512f,-0.780737221f,
-0.989176512f,-0.146730468f,-0.824589312f,0.565731823f,-0.219101235f,0.975702107f,0.50353837f,0.863972843f,-0.134580702f,0.990902662f,0.730562747f,-0.682845533f,
0.0674439222f,-0.997723043f,-0.632018745f,-0.774953127f,-0.990902662f,-0.134580702f,-0.815814435f,0.578313768f,-0.201104641f,0.979569793f,0.521975279f,0.852960587f,
-0.122410677f,0.992479563f,0.728464365f,-0.685083687f,0.061320737f,-0.998118103f,-0.639124453f,-0.769103348f,-0.992479563f,-0.122410677f,-0.806847572f,0.590759695f,
-0.183039889f,0.983105481f,0.540171444f,0.841554999f,-0.110222206f,0.993906975f,0.726359129f,-0.687315345f,0.0551952459f,-0.998475552f,-0.64617604f,-0.763188422f,
-0.993906975f,-0.110222206f,-0.797690868f,0.603066623f,-0.164913118f,0.986308098f,0.558118522f,0.829761207f,-0.0980171412f,0.99518472f,0.724247098f,-0.689540565f,
0.0490676761f,-0.99879545f,-0.653172851f,-0.757208824f,-0.99518472f,-0.0980171412f,-0.78834641f,0.615231574f,-0.146730468f,0.989176512f,0.575808167f,0.817584813f,
-0.0857973099f,0.996312618f,0.722128212f,-0.691759229f,0.0429382585f,-0.999077737f,-0.660114348f,-0.751165152f,-0.996312618f,-0.0857973099f,-0.778816521f,0.627251804f,
-0.128498107f,0.991709769f,0.593232274f,0.805031359f,-0.0735645667f,0.997290432f,0.720002532f,-0.693971455f,0.0368072242f,-0.999322355f,-0.666999936f,-0.745057762f,
-0.997290432f,-0.0735645667f,-0.769103348f,0.639124453f,-0.110222206f,0.993906975f,0.610382795f,0.792106569f,-0.061320737f,0.998118103f,0.717870057f,-0.696177125f,
0.030674804f,-0.999529421f,-0.673829019f,-0.73888731f,-0.998118103f,-0.061320737f,-0.759209216f,0.65084666f,-0.0919089541f,0.995767415f,0.627251804f,0.778816521f,
-0.0490676761f,0.99879545f,0.715730846f,-0.698376238f,0.024541229f,-0.999698818f,-0.680601001f,-0.732654274f,-0.99879545f,-0.0490676761f,-0.749136388f,0.662415802f,
-0.0735645667f,0.997290432f,0.643831551f,0.765167236f,-0.0368072242f,0.999322355f,0.71358484f,-0.700568795f,0.0184067301f,-0.999830604f,-0.687315345f,-0.726359129f,
-0.999322355f,-0.0368072242f,-0.73888731f,0.673829019f,-0.0551952459f,0.998475552f,0.660114348f,0.751165152f,-0.024541229f,0.999698818f,0.711432219f,-0.702754736f,
0.0122715384f,-0.999924719f,-0.693971455f,-0.720002532f,-0.999698818f,-0.024541229f,-0.728464365f,0.685083687f,-0.0368072242f,0.999322355f,0.676092684f,0.736816585f,
-0.0122715384f,0.999924719f,0.709272802f,-0.704934061f,0.00613588467f,-0.999981165f,-0.700568795f,-0.71358484f,-0.999924719f,-0.0122715384f,-0.717870057f,0.696177125f,
-0.0184067301f,0.999830604f,0.691759229f,0.722128212f
};
static __device__ const __attribute__((aligned(128))) float TWD[1280] = {
1.0f,0.0f,1.0f,0.0f,1.0f,0.0f,1.0f,0.0f,0.99995178f,-0.00981731899f,0.999807239f,-0.0196336918f,
0.999566317f,-0.0294481739f,1.0f,0.0f,0.999807239f,-0.0196336918f,0.999229014f,-0.0392598175f,0.998265624f,-0.0588708036f,1.0f,0.0f,
0.999566317f,-0.0294481739f,0.998265624f,-0.0588708036f,0.996099055f,-0.0882423669f,1.0f,0.0f,0.999229014f,-0.0392598175f,0.996917307f,-0.0784590989f,
0.993068457f,-0.117537394f,1.0f,0.0f,0.99879545f,-0.0490676761f,0.99518472f,-0.0980171412f,0.989176512f,-0.146730468f,1.0f,0.0f,
0.998265624f,-0.0588708036f,0.993068457f,-0.117537394f,0.984426558f,-0.175796285f,1.0f,0.0f,0.997639537f,-0.0686682612f,0.990569353f,-0.137012348f,
0.978822768f,-0.204709604f,1.0f,0.0f,0.996917307f,-0.0784590989f,0.987688363f,-0.156434461f,0.972369909f,-0.233445361f,1.0f,0.0f,
0.996099055f,-0.0882423669f,0.984426558f,-0.175796285f,0.965073645f,-0.261978626f,1.0f,0.0f,0.99518472f,-0.0980171412f,0.980785251f,-0.195090324f,
0.956940353f,-0.290284663f,1.0f,0.0f,0.99417448f,-0.107782461f,0.976765871f,-0.214309156f,0.947976947f,-0.318338931f,1.0f,0.0f,
0.993068457f,-0.117537394f,0.972369909f,-0.233445361f,0.938191354f,-0.346117049f,1.0f,0.0f,0.991866708f,-0.12728101f,0.967599094f,-0.252491564f,
0.92759192f,-0.37359497f,1.0f,0.0f,0.990569353f,-0.137012348f,0.962455213f,-0.271440446f,0.916187942f,-0.400748819f,1.0f,0.0f,
0.989176512f,-0.146730468f,0.956940353f,-0.290284663f,0.903989315f,-0.427555084f,1.0f,0.0f,0.987688363f,-0.156434461f,0.95105654f,-0.309017003f,
0.891006529f,-0.453990489f,1.0f,0.0f,0.986104965f,-0.166123375f,0.944806039f,-0.327630192f,0.87725091f,-0.480032116f,1.0f,0.0f,
0.984426558f,-0.175796285f,0.938191354f,-0.346117049f,0.862734377f,-0.505657375f,1.0f,0.0f,0.98265326f,-0.185452238f,0.931214929f,-0.364470512f,
0.847469568f,-0.530844033f,1.0f,0.0f,0.980785251f,-0.195090324f,0.923879504f,-0.382683426f,0.831469595f,-0.555570245f,1.0f,0.0f,
0.978822768f,-0.204709604f,0.916187942f,-0.400748819f,0.814748466f,-0.579814553f,1.0f,0.0f,0.976765871f,-0.214309156f,0.908143163f,-0.418659747f,
0.797320664f,-0.603555918f,1.0f,0.0f,0.974614859f,-0.223888054f,0.899748266f,-0.436409235f,0.779201269f,-0.626773834f,1.0f,0.0f,
0.972369909f,-0.233445361f,0.891006529f,-0.453990489f,0.760405958f,-0.649448037f,1.0f,0.0f,0.970031261f,-0.242980182f,0.881921291f,-0.471396744f,
0.740951121f,-0.671558976f,1.0f,0.0f,0.967599094f,-0.252491564f,0.872496009f,-0.488621235f,0.720853567f,-0.693087339f,1.0f,0.0f,
0.965073645f,-0.261978626f,0.862734377f,-0.505657375f,0.70013082f,-0.71401459f,1.0f,0.0f,0.962455213f,-0.271440446f,0.852640152f,-0.522498548f,
0.678800762f,-0.734322488f,1.0f,0.0f,0.959744036f,-0.2808761f,0.842217207f,-0.539138317f,0.656881928f,-0.753993511f,1.0f,0.0f,
0.956940353f,-0.290284663f,0.831469595f,-0.555570245f,0.634393275f,-0.773010433f,1.0f,0.0f,0.954044402f,-0.299665272f,0.82040143f,-0.571787953f,
0.611354411f,-0.791356921f,1.0f,0.0f,0.95105654f,-0.309017003f,0.809017003f,-0.587785244f,0.587785244f,-0.809017003f,1.0f,0.0f,
0.947976947f,-0.318338931f,0.797320664f,-0.603555918f,0.563706279f,-0.825975358f,1.0f,0.0f,0.944806039f,-0.327630192f,0.785316944f,-0.619093955f,
0.539138317f,-0.842217207f,1.0f,0.0f,0.941544056f,-0.336889863f,0.773010433f,-0.634393275f,0.514102757f,-0.857728601f,1.0f,0.0f,
0.938191354f,-0.346117049f,0.760405958f,-0.649448037f,0.488621235f,-0.872496009f,1.0f,0.0f,0.934748173f,-0.355310887f,0.747508347f,-0.66425246f,
0.462715924f,-0.886506617f,1.0f,0.0f,0.931214929f,-0.364470512f,0.734322488f,-0.678800762f,0.436409235f,-0.899748266f,1.0f,0.0f,
0.92759192f,-0.37359497f,0.720853567f,-0.693087339f,0.409724027f,-0.912209511f,1.0f,0.0f,0.923879504f,-0.382683426f,0.707106769f,-0.707106769f,
0.382683426f,-0.923879504f,1.0f,0.0f,0.920078099f,-0.391735017f,0.693087339f,-0.720853567f,0.355310887f,-0.934748173f,1.0f,0.0f,
0.916187942f,-0.400748819f,0.678800762f,-0.734322488f,0.327630192f,-0.944806039f,1.0f,0.0f,0.912209511f,-0.409724027f,0.66425246f,-0.747508347f,
0.299665272f,-0.954044402f,1.0f,0.0f,0.908143163f,-0.418659747f,0.649448037f,-0.760405958f,0.271440446f,-0.962455213f,1.0f,0.0f,
0.903989315f,-0.427555084f,0.634393275f,-0.773010433f,0.242980182f,-0.970031261f,1.0f,0.0f,0.899748266f,-0.436409235f,0.619093955f,-0.785316944f,
0.214309156f,-0.976765871f,1.0f,0.0f,0.895420551f,-0.445221335f,0.603555918f,-0.797320664f,0.185452238f,-0.98265326f,1.0f,0.0f,
0.891006529f,-0.453990489f,0.587785244f,-0.809017003f,0.156434461f,-0.987688363f,1.0f,0.0f,0.886506617f,-0.462715924f,0.571787953f,-0.82040143f,
0.12728101f,-0.991866708f,1.0f,0.0f,0.881921291f,-0.471396744f,0.555570245f,-0.831469595f,0.0980171412f,-0.99518472f,1.0f,0.0f,
0.87725091f,-0.480032116f,0.539138317f,-0.842217207f,0.0686682612f,-0.997639537f,1.0f,0.0f,0.872496009f,-0.488621235f,0.522498548f,-0.852640152f,
0.0392598175f,-0.999229014f,1.0f,0.0f,0.867657006f,-0.497163266f,0.505657375f,-0.862734377f,0.00981731899f,-0.99995178f,1.0f,0.0f,
0.862734377f,-0.505657375f,0.488621235f,-0.872496009f,-0.0196336918f,-0.999807239f,1.0f,0.0f,0.857728601f,-0.514102757f,0.471396744f,-0.881921291f,
-0.0490676761f,-0.99879545f,1.0f,0.0f,0.852640152f,-0.522498548f,0.453990489f,-0.891006529f,-0.0784590989f,-0.996917307f,1.0f,0.0f,
0.847469568f,-0.530844033f,0.436409235f,-0.899748266f,-0.107782461f,-0.99417448f,1.0f,0.0f,0.842217207f,-0.539138317f,0.418659747f,-0.908143163f,
-0.137012348f,-0.990569353f,1.0f,0.0f,0.836883724f,-0.547380686f,0.400748819f,-0.916187942f,-0.166123375f,-0.986104965f,1.0f,0.0f,
0.831469595f,-0.555570245f,0.382683426f,-0.923879504f,-0.195090324f,-0.980785251f,1.0f,0.0f,0.825975358f,-0.563706279f,0.364470512f,-0.931214929f,
-0.223888054f,-0.974614859f,1.0f,0.0f,0.82040143f,-0.571787953f,0.346117049f,-0.938191354f,-0.252491564f,-0.967599094f,1.0f,0.0f,
0.814748466f,-0.579814553f,0.327630192f,-0.944806039f,-0.2808761f,-0.959744036f,1.0f,0.0f,0.809017003f,-0.587785244f,0.309017003f,-0.95105654f,
-0.309017003f,-0.95105654f,1.0f,0.0f,0.803207517f,-0.59569931f,0.290284663f,-0.956940353f,-0.336889863f,-0.941544056f,1.0f,0.0f,
0.797320664f,-0.603555918f,0.271440446f,-0.962455213f,-0.364470512f,-0.931214929f,1.0f,0.0f,0.791356921f,-0.611354411f,0.252491564f,-0.967599094f,
-0.391735017f,-0.920078099f,1.0f,0.0f,0.785316944f,-0.619093955f,0.233445361f,-0.972369909f,-0.418659747f,-0.908143163f,1.0f,0.0f,
0.779201269f,-0.626773834f,0.214309156f,-0.976765871f,-0.445221335f,-0.895420551f,1.0f,0.0f,0.773010433f,-0.634393275f,0.195090324f,-0.980785251f,
-0.471396744f,-0.881921291f,1.0f,0.0f,0.76674515f,-0.641951621f,0.175796285f,-0.984426558f,-0.497163266f,-0.867657006f,1.0f,0.0f,
0.760405958f,-0.649448037f,0.156434461f,-0.987688363f,-0.522498548f,-0.852640152f,1.0f,0.0f,0.753993511f,-0.656881928f,0.137012348f,-0.990569353f,
-0.547380686f,-0.836883724f,1.0f,0.0f,0.747508347f,-0.66425246f,0.117537394f,-0.993068457f,-0.571787953f,-0.82040143f,1.0f,0.0f,
0.740951121f,-0.671558976f,0.0980171412f,-0.99518472f,-0.59569931f,-0.803207517f,1.0f,0.0f,0.734322488f,-0.678800762f,0.0784590989f,-0.996917307f,
-0.619093955f,-0.785316944f,1.0f,0.0f,0.727623105f,-0.685977101f,0.0588708036f,-0.998265624f,-0.641951621f,-0.76674515f,1.0f,0.0f,
0.720853567f,-0.693087339f,0.0392598175f,-0.999229014f,-0.66425246f,-0.747508347f,1.0f,0.0f,0.71401459f,-0.70013082f,0.0196336918f,-0.999807239f,
-0.685977101f,-0.727623105f,1.0f,0.0f,0.707106769f,-0.707106769f,0.0f,-1.0f,-0.707106769f,-0.707106769f,1.0f,0.0f,
0.70013082f,-0.71401459f,-0.0196336918f,-0.999807239f,-0.727623105f,-0.685977101f,1.0f,0.0f,0.693087339f,-0.720853567f,-0.0392598175f,-0.999229014f,
-0.747508347f,-0.66425246f,1.0f,0.0f,0.685977101f,-0.727623105f,-0.0588708036f,-0.998265624f,-0.76674515f,-0.641951621f,1.0f,0.0f,
0.678800762f,-0.734322488f,-0.0784590989f,-0.996917307f,-0.785316944f,-0.619093955f,1.0f,0.0f,0.671558976f,-0.740951121f,-0.0980171412f,-0.99518472f,
-0.803207517f,-0.59569931f,1.0f,0.0f,0.66425246f,-0.747508347f,-0.117537394f,-0.993068457f,-0.82040143f,-0.571787953f,1.0f,0.0f,
0.656881928f,-0.753993511f,-0.137012348f,-0.990569353f,-0.836883724f,-0.547380686f,1.0f,0.0f,0.649448037f,-0.760405958f,-0.156434461f,-0.987688363f,
-0.852640152f,-0.522498548f,1.0f,0.0f,0.641951621f,-0.76674515f,-0.175796285f,-0.984426558f,-0.867657006f,-0.497163266f,1.0f,0.0f,
0.634393275f,-0.773010433f,-0.195090324f,-0.980785251f,-0.881921291f,-0.471396744f,1.0f,0.0f,0.626773834f,-0.779201269f,-0.214309156f,-0.976765871f,
-0.895420551f,-0.445221335f,1.0f,0.0f,0.619093955f,-0.785316944f,-0.233445361f,-0.972369909f,-0.908143163f,-0.418659747f,1.0f,0.0f,
0.611354411f,-0.791356921f,-0.252491564f,-0.967599094f,-0.920078099f,-0.391735017f,1.0f,0.0f,0.603555918f,-0.797320664f,-0.271440446f,-0.962455213f,
-0.931214929f,-0.364470512f,1.0f,0.0f,0.59569931f,-0.803207517f,-0.290284663f,-0.956940353f,-0.941544056f,-0.336889863f,1.0f,0.0f,
0.587785244f,-0.809017003f,-0.309017003f,-0.95105654f,-0.95105654f,-0.309017003f,1.0f,0.0f,0.579814553f,-0.814748466f,-0.327630192f,-0.944806039f,
-0.959744036f,-0.2808761f,1.0f,0.0f,0.571787953f,-0.82040143f,-0.346117049f,-0.938191354f,-0.967599094f,-0.252491564f,1.0f,0.0f,
0.563706279f,-0.825975358f,-0.364470512f,-0.931214929f,-0.974614859f,-0.223888054f,1.0f,0.0f,0.555570245f,-0.831469595f,-0.382683426f,-0.923879504f,
-0.980785251f,-0.195090324f,1.0f,0.0f,0.547380686f,-0.836883724f,-0.400748819f,-0.916187942f,-0.986104965f,-0.166123375f,1.0f,0.0f,
0.539138317f,-0.842217207f,-0.418659747f,-0.908143163f,-0.990569353f,-0.137012348f,1.0f,0.0f,0.530844033f,-0.847469568f,-0.436409235f,-0.899748266f,
-0.99417448f,-0.107782461f,1.0f,0.0f,0.522498548f,-0.852640152f,-0.453990489f,-0.891006529f,-0.996917307f,-0.0784590989f,1.0f,0.0f,
0.514102757f,-0.857728601f,-0.471396744f,-0.881921291f,-0.99879545f,-0.0490676761f,1.0f,0.0f,0.505657375f,-0.862734377f,-0.488621235f,-0.872496009f,
-0.999807239f,-0.0196336918f,1.0f,0.0f,0.497163266f,-0.867657006f,-0.505657375f,-0.862734377f,-0.99995178f,0.00981731899f,1.0f,0.0f,
0.488621235f,-0.872496009f,-0.522498548f,-0.852640152f,-0.999229014f,0.0392598175f,1.0f,0.0f,0.480032116f,-0.87725091f,-0.539138317f,-0.842217207f,
-0.997639537f,0.0686682612f,1.0f,0.0f,0.471396744f,-0.881921291f,-0.555570245f,-0.831469595f,-0.99518472f,0.0980171412f,1.0f,0.0f,
0.462715924f,-0.886506617f,-0.571787953f,-0.82040143f,-0.991866708f,0.12728101f,1.0f,0.0f,0.453990489f,-0.891006529f,-0.587785244f,-0.809017003f,
-0.987688363f,0.156434461f,1.0f,0.0f,0.445221335f,-0.895420551f,-0.603555918f,-0.797320664f,-0.98265326f,0.185452238f,1.0f,0.0f,
0.436409235f,-0.899748266f,-0.619093955f,-0.785316944f,-0.976765871f,0.214309156f,1.0f,0.0f,0.427555084f,-0.903989315f,-0.634393275f,-0.773010433f,
-0.970031261f,0.242980182f,1.0f,0.0f,0.418659747f,-0.908143163f,-0.649448037f,-0.760405958f,-0.962455213f,0.271440446f,1.0f,0.0f,
0.409724027f,-0.912209511f,-0.66425246f,-0.747508347f,-0.954044402f,0.299665272f,1.0f,0.0f,0.400748819f,-0.916187942f,-0.678800762f,-0.734322488f,
-0.944806039f,0.327630192f,1.0f,0.0f,0.391735017f,-0.920078099f,-0.693087339f,-0.720853567f,-0.934748173f,0.355310887f,1.0f,0.0f,
0.382683426f,-0.923879504f,-0.707106769f,-0.707106769f,-0.923879504f,0.382683426f,1.0f,0.0f,0.37359497f,-0.92759192f,-0.720853567f,-0.693087339f,
-0.912209511f,0.409724027f,1.0f,0.0f,0.364470512f,-0.931214929f,-0.734322488f,-0.678800762f,-0.899748266f,0.436409235f,1.0f,0.0f,
0.355310887f,-0.934748173f,-0.747508347f,-0.66425246f,-0.886506617f,0.462715924f,1.0f,0.0f,0.346117049f,-0.938191354f,-0.760405958f,-0.649448037f,
-0.872496009f,0.488621235f,1.0f,0.0f,0.336889863f,-0.941544056f,-0.773010433f,-0.634393275f,-0.857728601f,0.514102757f,1.0f,0.0f,
0.327630192f,-0.944806039f,-0.785316944f,-0.619093955f,-0.842217207f,0.539138317f,1.0f,0.0f,0.318338931f,-0.947976947f,-0.797320664f,-0.603555918f,
-0.825975358f,0.563706279f,1.0f,0.0f,0.309017003f,-0.95105654f,-0.809017003f,-0.587785244f,-0.809017003f,0.587785244f,1.0f,0.0f,
0.299665272f,-0.954044402f,-0.82040143f,-0.571787953f,-0.791356921f,0.611354411f,1.0f,0.0f,0.290284663f,-0.956940353f,-0.831469595f,-0.555570245f,
-0.773010433f,0.634393275f,1.0f,0.0f,0.2808761f,-0.959744036f,-0.842217207f,-0.539138317f,-0.753993511f,0.656881928f,1.0f,0.0f,
0.271440446f,-0.962455213f,-0.852640152f,-0.522498548f,-0.734322488f,0.678800762f,1.0f,0.0f,0.261978626f,-0.965073645f,-0.862734377f,-0.505657375f,
-0.71401459f,0.70013082f,1.0f,0.0f,0.252491564f,-0.967599094f,-0.872496009f,-0.488621235f,-0.693087339f,0.720853567f,1.0f,0.0f,
0.242980182f,-0.970031261f,-0.881921291f,-0.471396744f,-0.671558976f,0.740951121f,1.0f,0.0f,0.233445361f,-0.972369909f,-0.891006529f,-0.453990489f,
-0.649448037f,0.760405958f,1.0f,0.0f,0.223888054f,-0.974614859f,-0.899748266f,-0.436409235f,-0.626773834f,0.779201269f,1.0f,0.0f,
0.214309156f,-0.976765871f,-0.908143163f,-0.418659747f,-0.603555918f,0.797320664f,1.0f,0.0f,0.204709604f,-0.978822768f,-0.916187942f,-0.400748819f,
-0.579814553f,0.814748466f,1.0f,0.0f,0.195090324f,-0.980785251f,-0.923879504f,-0.382683426f,-0.555570245f,0.831469595f,1.0f,0.0f,
0.185452238f,-0.98265326f,-0.931214929f,-0.364470512f,-0.530844033f,0.847469568f,1.0f,0.0f,0.175796285f,-0.984426558f,-0.938191354f,-0.346117049f,
-0.505657375f,0.862734377f,1.0f,0.0f,0.166123375f,-0.986104965f,-0.944806039f,-0.327630192f,-0.480032116f,0.87725091f,1.0f,0.0f,
0.156434461f,-0.987688363f,-0.95105654f,-0.309017003f,-0.453990489f,0.891006529f,1.0f,0.0f,0.146730468f,-0.989176512f,-0.956940353f,-0.290284663f,
-0.427555084f,0.903989315f,1.0f,0.0f,0.137012348f,-0.990569353f,-0.962455213f,-0.271440446f,-0.400748819f,0.916187942f,1.0f,0.0f,
0.12728101f,-0.991866708f,-0.967599094f,-0.252491564f,-0.37359497f,0.92759192f,1.0f,0.0f,0.117537394f,-0.993068457f,-0.972369909f,-0.233445361f,
-0.346117049f,0.938191354f,1.0f,0.0f,0.107782461f,-0.99417448f,-0.976765871f,-0.214309156f,-0.318338931f,0.947976947f,1.0f,0.0f,
0.0980171412f,-0.99518472f,-0.980785251f,-0.195090324f,-0.290284663f,0.956940353f,1.0f,0.0f,0.0882423669f,-0.996099055f,-0.984426558f,-0.175796285f,
-0.261978626f,0.965073645f,1.0f,0.0f,0.0784590989f,-0.996917307f,-0.987688363f,-0.156434461f,-0.233445361f,0.972369909f,1.0f,0.0f,
0.0686682612f,-0.997639537f,-0.990569353f,-0.137012348f,-0.204709604f,0.978822768f,1.0f,0.0f,0.0588708036f,-0.998265624f,-0.993068457f,-0.117537394f,
-0.175796285f,0.984426558f,1.0f,0.0f,0.0490676761f,-0.99879545f,-0.99518472f,-0.0980171412f,-0.146730468f,0.989176512f,1.0f,0.0f,
0.0392598175f,-0.999229014f,-0.996917307f,-0.0784590989f,-0.117537394f,0.993068457f,1.0f,0.0f,0.0294481739f,-0.999566317f,-0.998265624f,-0.0588708036f,
-0.0882423669f,0.996099055f,1.0f,0.0f,0.0196336918f,-0.999807239f,-0.999229014f,-0.0392598175f,-0.0588708036f,0.998265624f,1.0f,0.0f,
0.00981731899f,-0.99995178f,-0.999807239f,-0.0196336918f,-0.0294481739f,0.999566317f,1.0f,0.0f
};
static __device__ const __attribute__((aligned(128))) float TWR[320] = {
1.0f,0.0f,1.0f,0.0f,1.0f,0.0f,1.0f,0.0f,1.0f,0.0f,1.0f,0.0f,
1.0f,0.0f,1.0f,0.0f,1.0f,0.0f,1.0f,0.0f,0.999229014f,-0.0392598175f,0.996917307f,-0.0784590989f,
0.993068457f,-0.117537394f,0.987688363f,-0.156434461f,0.980785251f,-0.195090324f,0.972369909f,-0.233445361f,0.962455213f,-0.271440446f,0.95105654f,-0.309017003f,
0.938191354f,-0.346117049f,1.0f,0.0f,0.996917307f,-0.0784590989f,0.987688363f,-0.156434461f,0.972369909f,-0.233445361f,0.95105654f,-0.309017003f,
0.923879504f,-0.382683426f,0.891006529f,-0.453990489f,0.852640152f,-0.522498548f,0.809017003f,-0.587785244f,0.760405958f,-0.649448037f,1.0f,0.0f,
0.993068457f,-0.117537394f,0.972369909f,-0.233445361f,0.938191354f,-0.346117049f,0.891006529f,-0.453990489f,0.831469595f,-0.555570245f,0.760405958f,-0.649448037f,
0.678800762f,-0.734322488f,0.587785244f,-0.809017003f,0.488621235f,-0.872496009f,1.0f,0.0f,0.987688363f,-0.156434461f,0.95105654f,-0.309017003f,
0.891006529f,-0.453990489f,0.809017003f,-0.587785244f,0.707106769f,-0.707106769f,0.587785244f,-0.809017003f,0.453990489f,-0.891006529f,0.309017003f,-0.95105654f,
0.156434461f,-0.987688363f,1.0f,0.0f,0.980785251f,-0.195090324f,0.923879504f,-0.382683426f,0.831469595f,-0.555570245f,0.707106769f,-0.707106769f,
0.555570245f,-0.831469595f,0.382683426f,-0.923879504f,0.195090324f,-0.980785251f,0.0f,-1.0f,-0.195090324f,-0.980785251f,1.0f,0.0f,
0.972369909f,-0.233445361f,0.891006529f,-0.453990489f,0.760405958f,-0.649448037f,0.587785244f,-0.809017003f,0.382683426f,-0.923879504f,0.156434461f,-0.987688363f,
-0.0784590989f,-0.996917307f,-0.309017003f,-0.95105654f,-0.522498548f,-0.852640152f,1.0f,0.0f,0.962455213f,-0.271440446f,0.852640152f,-0.522498548f,
0.678800762f,-0.734322488f,0.453990489f,-0.891006529f,0.195090324f,-0.980785251f,-0.0784590989f,-0.996917307f,-0.346117049f,-0.938191354f,-0.587785244f,-0.809017003f,
-0.785316944f,-0.619093955f,1.0f,0.0f,0.95105654f,-0.309017003f,0.809017003f,-0.587785244f,0.587785244f,-0.809017003f,0.309017003f,-0.95105654f,
0.0f,-1.0f,-0.309017003f,-0.95105654f,-0.587785244f,-0.809017003f,-0.809017003f,-0.587785244f,-0.95105654f,-0.309017003f,1.0f,0.0f,
0.938191354f,-0.346117049f,0.760405958f,-0.649448037f,0.488621235f,-0.872496009f,0.156434461f,-0.987688363f,-0.195090324f,-0.980785251f,-0.522498548f,-0.852640152f,
-0.785316944f,-0.619093955f,-0.95105654f,-0.309017003f,-0.999229014f,0.0392598175f,1.0f,0.0f,0.923879504f,-0.382683426f,0.707106769f,-0.707106769f,
0.382683426f,-0.923879504f,0.0f,-1.0f,-0.382683426f,-0.923879504f,-0.707106769f,-0.707106769f,-0.923879504f,-0.382683426f,-1.0f,0.0f,
-0.923879504f,0.382683426f,1.0f,0.0f,0.908143163f,-0.418659747f,0.649448037f,-0.760405958f,0.271440446f,-0.962455213f,-0.156434461f,-0.987688363f,
-0.555570245f,-0.831469595f,-0.852640152f,-0.522498548f,-0.993068457f,-0.117537394f,-0.95105654f,0.309017003f,-0.734322488f,0.678800762f,1.0f,0.0f,
0.891006529f,-0.453990489f,0.587785244f,-0.809017003f,0.156434461f,-0.987688363f,-0.309017003f,-0.95105654f,-0.707106769f,-0.707106769f,-0.95105654f,-0.309017003f,
-0.987688363f,0.156434461f,-0.809017003f,0.587785244f,-0.453990489f,0.891006529f,1.0f,0.0f,0.872496009f,-0.488621235f,0.522498548f,-0.852640152f,
0.0392598175f,-0.999229014f,-0.453990489f,-0.891006529f,-0.831469595f,-0.555570245f,-0.996917307f,-0.0784590989f,-0.908143163f,0.418659747f,-0.587785244f,0.809017003f,
-0.117537394f,0.993068457f,1.0f,0.0f,0.852640152f,-0.522498548f,0.453990489f,-0.891006529f,-0.0784590989f,-0.996917307f,-0.587785244f,-0.809017003f,
-0.923879504f,-0.382683426f,-0.987688363f,0.156434461f,-0.760405958f,0.649448037f,-0.309017003f,0.95105654f,0.233445361f,0.972369909f,1.0f,0.0f,
0.831469595f,-0.555570245f,0.382683426f,-0.923879504f,-0.195090324f,-0.980785251f,-0.707106769f,-0.707106769f,-0.980785251f,-0.195090324f,-0.923879504f,0.382683426f,
-0.555570245f,0.831469595f,0.0f,1.0f,0.555570245f,0.831469595f,1.0f,0.0f
};
static __device__ const __attribute__((aligned(128))) float TWC[512] = {
1.0f,0.0f,1.0f,0.0f,1.0f,0.0f,1.0f,0.0f,1.0f,0.0f,1.0f,0.0f,
1.0f,0.0f,1.0f,0.0f,1.0f,0.0f,1.0f,0.0f,1.0f,0.0f,1.0f,0.0f,
1.0f,0.0f,1.0f,0.0f,1.0f,0.0f,1.0f,0.0f,1.0f,0.0f,0.999698818f,-0.024541229f,
0.99879545f,-0.0490676761f,0.997290432f,-0.0735645667f,0.99518472f,-0.0980171412f,0.992479563f,-0.122410677f,0.989176512f,-0.146730468f,0.985277653f,-0.170961887f,
0.980785251f,-0.195090324f,0.975702107f,-0.219101235f,0.970031261f,-0.242980182f,0.963776052f,-0.266712755f,0.956940353f,-0.290284663f,0.949528158f,-0.313681751f,
0.941544056f,-0.336889863f,0.932992816f,-0.359895051f,1.0f,0.0f,0.99879545f,-0.0490676761f,0.99518472f,-0.0980171412f,0.989176512f,-0.146730468f,
0.980785251f,-0.195090324f,0.970031261f,-0.242980182f,0.956940353f,-0.290284663f,0.941544056f,-0.336889863f,0.923879504f,-0.382683426f,0.903989315f,-0.427555084f,
0.881921291f,-0.471396744f,0.857728601f,-0.514102757f,0.831469595f,-0.555570245f,0.803207517f,-0.59569931f,0.773010433f,-0.634393275f,0.740951121f,-0.671558976f,
1.0f,0.0f,0.997290432f,-0.0735645667f,0.989176512f,-0.146730468f,0.975702107f,-0.219101235f,0.956940353f,-0.290284663f,0.932992816f,-0.359895051f,
0.903989315f,-0.427555084f,0.870086968f,-0.492898196f,0.831469595f,-0.555570245f,0.78834641f,-0.615231574f,0.740951121f,-0.671558976f,0.689540565f,-0.724247098f,
0.634393275f,-0.773010433f,0.575808167f,-0.817584813f,0.514102757f,-0.857728601f,0.449611336f,-0.893224299f,1.0f,0.0f,0.99518472f,-0.0980171412f,
0.980785251f,-0.195090324f,0.956940353f,-0.290284663f,0.923879504f,-0.382683426f,0.881921291f,-0.471396744f,0.831469595f,-0.555570245f,0.773010433f,-0.634393275f,
0.707106769f,-0.707106769f,0.634393275f,-0.773010433f,0.555570245f,-0.831469595f,0.471396744f,-0.881921291f,0.382683426f,-0.923879504f,0.290284663f,-0.956940353f,
0.195090324f,-0.980785251f,0.0980171412f,-0.99518472f,1.0f,0.0f,0.992479563f,-0.122410677f,0.970031261f,-0.242980182f,0.932992816f,-0.359895051f,
0.881921291f,-0.471396744f,0.817584813f,-0.575808167f,0.740951121f,-0.671558976f,0.653172851f,-0.757208824f,0.555570245f,-0.831469595f,0.449611336f,-0.893224299f,
0.336889863f,-0.941544056f,0.219101235f,-0.975702107f,0.0980171412f,-0.99518472f,-0.024541229f,-0.999698818f,-0.146730468f,-0.989176512f,-0.266712755f,-0.963776052f,
1.0f,0.0f,0.989176512f,-0.146730468f,0.956940353f,-0.290284663f,0.903989315f,-0.427555084f,0.831469595f,-0.555570245f,0.740951121f,-0.671558976f,
0.634393275f,-0.773010433f,0.514102757f,-0.857728601f,0.382683426f,-0.923879504f,0.242980182f,-0.970031261f,0.0980171412f,-0.99518472f,-0.0490676761f,-0.99879545f,
-0.195090324f,-0.980785251f,-0.336889863f,-0.941544056f,-0.471396744f,-0.881921291f,-0.59569931f,-0.803207517f,1.0f,0.0f,0.985277653f,-0.170961887f,
0.941544056f,-0.336889863f,0.870086968f,-0.492898196f,0.773010433f,-0.634393275f,0.653172851f,-0.757208824f,0.514102757f,-0.857728601f,0.359895051f,-0.932992816f,
0.195090324f,-0.980785251f,0.024541229f,-0.999698818f,-0.146730468f,-0.989176512f,-0.313681751f,-0.949528158f,-0.471396744f,-0.881921291f,-0.615231574f,-0.78834641f,
-0.740951121f,-0.671558976f,-0.84485358f,-0.534997642f,1.0f,0.0f,0.980785251f,-0.195090324f,0.923879504f,-0.382683426f,0.831469595f,-0.555570245f,
0.707106769f,-0.707106769f,0.555570245f,-0.831469595f,0.382683426f,-0.923879504f,0.195090324f,-0.980785251f,0.0f,-1.0f,-0.195090324f,-0.980785251f,
-0.382683426f,-0.923879504f,-0.555570245f,-0.831469595f,-0.707106769f,-0.707106769f,-0.831469595f,-0.555570245f,-0.923879504f,-0.382683426f,-0.980785251f,-0.195090324f,
1.0f,0.0f,0.975702107f,-0.219101235f,0.903989315f,-0.427555084f,0.78834641f,-0.615231574f,0.634393275f,-0.773010433f,0.449611336f,-0.893224299f,
0.242980182f,-0.970031261f,0.024541229f,-0.999698818f,-0.195090324f,-0.980785251f,-0.405241311f,-0.914209783f,-0.59569931f,-0.803207517f,-0.757208824f,-0.653172851f,
-0.881921291f,-0.471396744f,-0.963776052f,-0.266712755f,-0.99879545f,-0.0490676761f,-0.985277653f,0.170961887f,1.0f,0.0f,0.970031261f,-0.242980182f,
0.881921291f,-0.471396744f,0.740951121f,-0.671558976f,0.555570245f,-0.831469595f,0.336889863f,-0.941544056f,0.0980171412f,-0.99518472f,-0.146730468f,-0.989176512f,
-0.382683426f,-0.923879504f,-0.59569931f,-0.803207517f,-0.773010433f,-0.634393275f,-0.903989315f,-0.427555084f,-0.980785251f,-0.195090324f,-0.99879545f,0.0490676761f,
-0.956940353f,0.290284663f,-0.857728601f,0.514102757f,1.0f,0.0f,0.963776052f,-0.266712755f,0.857728601f,-0.514102757f,0.689540565f,-0.724247098f,
0.471396744f,-0.881921291f,0.219101235f,-0.975702107f,-0.0490676761f,-0.99879545f,-0.313681751f,-0.949528158f,-0.555570245f,-0.831469595f,-0.757208824f,-0.653172851f,
-0.903989315f,-0.427555084f,-0.985277653f,-0.170961887f,-0.99518472f,0.0980171412f,-0.932992816f,0.359895051f,-0.803207517f,0.59569931f,-0.615231574f,0.78834641f,
1.0f,0.0f,0.956940353f,-0.290284663f,0.831469595f,-0.555570245f,0.634393275f,-0.773010433f,0.382683426f,-0.923879504f,0.0980171412f,-0.99518472f,
-0.195090324f,-0.980785251f,-0.471396744f,-0.881921291f,-0.707106769f,-0.707106769f,-0.881921291f,-0.471396744f,-0.980785251f,-0.195090324f,-0.99518472f,0.0980171412f,
-0.923879504f,0.382683426f,-0.773010433f,0.634393275f,-0.555570245f,0.831469595f,-0.290284663f,0.956940353f,1.0f,0.0f,0.949528158f,-0.313681751f,
0.803207517f,-0.59569931f,0.575808167f,-0.817584813f,0.290284663f,-0.956940353f,-0.024541229f,-0.999698818f,-0.336889863f,-0.941544056f,-0.615231574f,-0.78834641f,
-0.831469595f,-0.555570245f,-0.963776052f,-0.266712755f,-0.99879545f,0.0490676761f,-0.932992816f,0.359895051f,-0.773010433f,0.634393275f,-0.534997642f,0.84485358f,
-0.242980182f,0.970031261f,0.0735645667f,0.997290432f,1.0f,0.0f,0.941544056f,-0.336889863f,0.773010433f,-0.634393275f,0.514102757f,-0.857728601f,
0.195090324f,-0.980785251f,-0.146730468f,-0.989176512f,-0.471396744f,-0.881921291f,-0.740951121f,-0.671558976f,-0.923879504f,-0.382683426f,-0.99879545f,-0.0490676761f,
-0.956940353f,0.290284663f,-0.803207517f,0.59569931f,-0.555570245f,0.831469595f,-0.242980182f,0.970031261f,0.0980171412f,0.99518472f,0.427555084f,0.903989315f,
1.0f,0.0f,0.932992816f,-0.359895051f,0.740951121f,-0.671558976f,0.449611336f,-0.893224299f,0.0980171412f,-0.99518472f,-0.266712755f,-0.963776052f,
-0.59569931f,-0.803207517f,-0.84485358f,-0.534997642f,-0.980785251f,-0.195090324f,-0.985277653f,0.170961887f,-0.857728601f,0.514102757f,-0.615231574f,0.78834641f,
-0.290284663f,0.956940353f,0.0735645667f,0.997290432f,0.427555084f,0.903989315f,0.724247098f,0.689540565f
};

#define STAMP_BEGIN()
#define STAMP(kid, i)
#define STAMP_END(kid)
#define STARG
#define STPASS
#define SARG
#define SPASS
#define DEV __device__ __forceinline__

DEV float bf_lo(unsigned v) { return __uint_as_float(v << 16); }
DEV float bf_hi(unsigned v) { return __uint_as_float(v & 0xffff0000u); }
DEV f2 bf_up(unsigned v) { return f2{bf_lo(v), bf_hi(v)}; }
DEV float rbf(float x) { return (float)(__bf16)x; }
DEV unsigned pack_bf(float lo, float hi) {
  bfx2 r = {(__bf16)lo, (__bf16)hi};
  return __builtin_bit_cast(unsigned, r);
}
DEV unsigned pack_bf2(f2 v) { return pack_bf(v.x, v.y); }
DEV f2 rbf2(f2 v) { return bf_up(pack_bf2(v)); }

#define PK2(op, mods, CB)                                                        \
  f2 d;                                                                          \
  asm(op " %0, %1, %2 " mods : "=v"(d) : "v"(a), CB(b));                         \
  return d;
#define PK3(mods, CB)                                                            \
  f2 d;                                                                          \
  asm("v_pk_fma_f32 %0, %1, %2, %3 " mods : "=v"(d) : "v"(a), CB(b), "v"(c));    \
  return d;
template <int SG> DEV f2 addi(f2 a, f2 b) {
  if (SG > 0) { PK2("v_pk_add_f32", "op_sel:[0,1] op_sel_hi:[1,0] neg_lo:[0,1]", "v") }
  else { PK2("v_pk_add_f32", "op_sel:[0,1] op_sel_hi:[1,0] neg_hi:[0,1]", "v") }
}
template <int SG> DEV f2 fmai(f2 a, f2 b, f2 c) {
  if (SG > 0) { PK3("op_sel:[1,0,0] op_sel_hi:[0,1,1] neg_lo:[1,0,0]", "s") }
  else { PK3("op_sel:[1,0,0] op_sel_hi:[0,1,1] neg_hi:[1,0,0]", "s") }
}
DEV f2 fmas(f2 a, f2 b, f2 c) { PK3("", "s") }
DEV f2 fmsn(f2 a, f2 b, f2 c) { PK3("neg_lo:[1,0,0] neg_hi:[1,0,0]", "s") }
DEV f2 fmsb(f2 a, f2 b, f2 c) { PK3("neg_lo:[0,0,1] neg_hi:[0,0,1]", "s") }
template <bool S> DEV f2 m1(f2 a, f2 b) {
  if (S) { PK2("v_pk_mul_f32", "op_sel_hi:[0,1]", "s") } else { PK2("v_pk_mul_f32", "op_sel_hi:[0,1]", "v") }
}
template <bool S> DEV f2 m1c(f2 a, f2 b) {
  if (S) { PK2("v_pk_mul_f32", "op_sel_hi:[0,1] neg_hi:[0,1]", "s") }
  else { PK2("v_pk_mul_f32", "op_sel_hi:[0,1] neg_hi:[0,1]", "v") }
}
template <bool S> DEV f2 m2(f2 a, f2 b, f2 c) {
  if (S) { PK3("op_sel:[1,1,0] op_sel_hi:[1,0,1] neg_lo:[0,1,0]", "s") }
  else { PK3("op_sel:[1,1,0] op_sel_hi:[1,0,1] neg_lo:[0,1,0]", "v") }
}
template <bool S> DEV f2 m2c(f2 a, f2 b, f2 c) {
  if (S) { PK3("op_sel:[1,1,0] op_sel_hi:[1,0,1]", "s") } else { PK3("op_sel:[1,1,0] op_sel_hi:[1,0,1]", "v") }
}
template <bool S> DEV f2 m2j(f2 a, f2 b, f2 c) {
  if (S) { PK3("op_sel:[1,1,0] op_sel_hi:[1,0,1] neg_hi:[0,1,0]", "s") }
  else { PK3("op_sel:[1,1,0] op_sel_hi:[1,0,1] neg_hi:[0,1,0]", "v") }
}
DEV f2 f1s(f2 a, f2 b, f2 c) { PK3("op_sel_hi:[0,1,1]", "s") }
DEV f2 f1cs(f2 a, f2 b, f2 c) { PK3("op_sel_hi:[0,1,1] neg_hi:[0,1,0]", "s") }
DEV f2 f1ns(f2 a, f2 b, f2 c) { PK3("op_sel_hi:[0,1,1] neg_lo:[1,0,0] neg_hi:[1,0,1]", "s") }
template <bool S = false> DEV f2 cmul(f2 a, f2 b) { return m2<S>(a, b, m1<S>(a, b)); }
template <bool S = false> DEV f2 cmulc(f2 a, f2 b) { return m2c<S>(a, b, m1c<S>(a, b)); }
template <bool S = false> DEV f2 cjmul(f2 a, f2 b) { return m2j<S>(a, b, m1<S>(a, b)); }
DEV f2 cfma_s(f2 a, f2 b, f2 c) { return m2<true>(a, b, f1s(a, b, c)); }
DEV f2 addc(f2 a, f2 b) { PK2("v_pk_add_f32", "neg_hi:[0,1]", "v") }
DEV f2 subc(f2 a, f2 b) { PK2("v_pk_add_f32", "neg_lo:[0,1]", "v") }
DEV f2 xx_pm_yy(f2 a, f2 b) { PK2("v_pk_add_f32", "op_sel:[0,1] op_sel_hi:[0,1] neg_hi:[0,1]", "v") }
DEV f2 xx_mp_yy(f2 a, f2 b) { PK2("v_pk_add_f32", "op_sel:[0,1] op_sel_hi:[0,1] neg_lo:[0,1]", "v") }
DEV f2 xx_pm_xx(f2 a, f2 b) { PK2("v_pk_add_f32", "op_sel:[0,0] op_sel_hi:[0,0] neg_hi:[0,1]", "v") }
DEV f2 yy_pm_xx(f2 a, f2 b) { PK2("v_pk_add_f32", "op_sel:[1,0] op_sel_hi:[1,0] neg_hi:[0,1]", "v") }
DEV f2 yy_mp_yy(f2 a, f2 b) { PK2("v_pk_add_f32", "op_sel:[1,1] op_sel_hi:[1,1] neg_lo:[0,1]", "v") }
DEV f2 fma_pm(f2 a, f2 b, f2 c) { PK3("neg_hi:[1,0,0]", "s") }
DEV f2 fma_mp(f2 a, f2 b, f2 c) { PK3("neg_lo:[1,0,0]", "s") }
DEV f2 fma_pmc(f2 a, f2 b, f2 c) { PK3("neg_hi:[1,0,1]", "s") }

template <int SG> DEV void dft4(f2& a, f2& b, f2& c, f2& d) {
  f2 t0 = a + c, t1 = a - c, t2 = b + d, t3 = b - d;
  a = t0 + t2; c = t0 - t2; b = addi<SG>(t1, t3); d = addi<-SG>(t1, t3);
}
template <int SG, bool P1 = true> DEV void dft16(f2 (&v)[16]) {
  const float c1 = 0.92387953251128674f, s1 = 0.38268343236508977f, r = 0.70710678118654752f;
  const f2 w1 = {c1, SG * s1}, w3 = {s1, SG * c1}, rr = {r, r}, two = {2.f, 2.f};
#pragma unroll
  for (int n2 = 0; n2 < 4; ++n2)
    if (P1) dft4<SG>(v[n2], v[4 + n2], v[8 + n2], v[12 + n2]);
  f2 o[16];
  {
    f2 t0 = v[0] + v[2], t1 = v[0] - v[2], t2 = v[1] + v[3], t3 = v[1] - v[3];
    o[0] = t0 + t2; o[8] = t0 - t2; o[4] = addi<SG>(t1, t3); o[12] = addi<-SG>(t1, t3);
  }
  {
    f2 s = addi<SG>(v[6], v[6]);
    f2 t0 = fmas(s, rr, v[4]), t1 = fmsn(s, rr, v[4]);
    f2 bb = cmul<true>(v[5], w1);
    f2 t2 = cfma_s(v[7], w3, bb), t3 = fmsb(bb, two, t2);
    o[1] = t0 + t2; o[9] = t0 - t2; o[5] = addi<SG>(t1, t3); o[13] = addi<-SG>(t1, t3);
  }
  {
    f2 t0 = addi<SG>(v[8], v[10]), t1 = addi<-SG>(v[8], v[10]);
    f2 p = v[9] + v[11], q = v[9] - v[11];
    f2 T2 = addi<SG>(q, p), T3 = addi<SG>(p, q);
    o[2] = fmas(T2, rr, t0); o[10] = fmsn(T2, rr, t0); o[6] = fmai<SG>(T3, rr, t1); o[14] = fmai<-SG>(T3, rr, t1);
  }
  {
    f2 s = addi<-SG>(v[14], v[14]);
    f2 t0 = fmsn(s, rr, v[12]), t1 = fmas(s, rr, v[12]);
    f2 bb = cmul<true>(v[13], w3);
    f2 t3 = cfma_s(v[15], w1, bb), t2 = fmsb(bb, two, t3);
    o[3] = t0 + t2; o[11] = t0 - t2; o[7] = addi<SG>(t1, t3); o[15] = addi<-SG>(t1, t3);
  }
#pragma unroll
  for (int i = 0; i < 16; ++i) v[i] = o[i];
}
template <int SG> DEV void dft5(f2& x0, f2& x1, f2& x2, f2& x3, f2& x4) {
  const float c1 = 0.30901699437494742f, c2 = -0.80901699437494742f;
  const float s1 = 0.95105651629515357f, q = 0.61803398874989485f;
  const f2 C1 = {c1, c1}, C2 = {c2, c2}, S1 = {s1, s1}, Q = {q, q};
  f2 t1 = x1 + x4, t2 = x2 + x3, t3 = x1 - x4, t4 = x2 - x3;
  f2 a1 = fmas(t2, C2, fmas(t1, C1, x0)), a2 = fmas(t2, C1, fmas(t1, C2, x0));
  f2 u1 = fmas(t4, Q, t3), u2 = fmsb(t3, Q, t4);
  x0 = x0 + t1 + t2;
  x1 = fmai<SG>(u1, S1, a1); x4 = fmai<-SG>(u1, S1, a1);
  x2 = fmai<SG>(u2, S1, a2); x3 = fmai<-SG>(u2, S1, a2);
}
template <int SG> DEV void dft10(f2 (&v)[10]) {
  f2 s0 = v[0] + v[5], d0 = v[0] - v[5];
  f2 s1 = v[2] + v[7], d1 = v[2] - v[7];
  f2 s2 = v[4] + v[9], d2 = v[4] - v[9];
  f2 s3 = v[6] + v[1], d3 = v[6] - v[1];
  f2 s4 = v[8] + v[3], d4 = v[8] - v[3];
  dft5<SG>(s0, s1, s2, s3, s4);
  dft5<SG>(d0, d1, d2, d3, d4);
  v[0] = s0; v[6] = s1; v[2] = s2; v[8] = s3; v[4] = s4;
  v[5] = d0; v[1] = d1; v[7] = d2; v[3] = d3; v[9] = d4;
}

template <int CTRL, int RM> DEV float dpp_mov(float v) {
  return __int_as_float(__builtin_amdgcn_update_dpp(0, __float_as_int(v), CTRL, RM, 0xf, true));
}
DEV void ln_row(const f2 (&xv)[5], unsigned (&o)[5]) {
  const float sh = __int_as_float(__builtin_amdgcn_readfirstlane(__float_as_int(xv[0].x)));
  f2 a[5];
#pragma unroll
  for (int i = 0; i < 5; ++i) a[i] = xv[i] - sh;
  f2 s1 = (a[0] + a[1]) + (a[2] + a[3]) + a[4];
  f2 s2 = a[0] * a[0] + a[1] * a[1];
  f2 s3 = a[2] * a[2] + a[3] * a[3];
  s2 = a[4] * a[4] + (s2 + s3);
  auto r = __builtin_amdgcn_permlane32_swap(__float_as_uint(s1.x + s1.y), __float_as_uint(s2.x + s2.y), false, false);
  float p = __uint_as_float(r[0]) + __uint_as_float(r[1]);
  p += dpp_mov<0xB1, 0xf>(p);
  p += dpp_mov<0x4E, 0xf>(p);
  p += dpp_mov<0x141, 0xf>(p);
  p += dpp_mov<0x140, 0xf>(p);
  p += dpp_mov<0x142, 0xa>(p);
  const float S1 = __int_as_float(__builtin_amdgcn_readlane(__float_as_int(p), 31));
  const float S2 = __int_as_float(__builtin_amdgcn_readlane(__float_as_int(p), 63));
  const float dm = S1 * (1.f / 640.f);
  const float var = fmaxf(S2 * (1.f / 640.f) - dm * dm, 0.f);
  const float sd = rbf(__builtin_amdgcn_sqrtf(rbf(rbf(var) + 1e-5f)));
  const float mb = rbf(sh + dm), ri = __builtin_amdgcn_rcpf(sd);
#pragma unroll
  for (int i = 0; i < 5; ++i) o[i] = pack_bf2(rbf2(xv[i] - mb) * ri);
}

#define SYM_ADDR(dst, sym)                                                                     \
  asm volatile("s_getpc_b64 s[40:41]\n s_add_u32 s40, s40, " #sym "@rel32@lo+4\n"              \
               "s_addc_u32 s41, s41, " #sym "@rel32@hi+12\n s_mov_b64 %0, s[40:41]"            \
               : "=s"(dst)::"s40", "s41", "scc")
DEV unsigned touch(const char* p) {
  unsigned v;
  asm volatile("global_load_dword %0, %1, off" : "=v"(v) : "v"(p));
  return v;
}
DEV void transpose8x8(const u4 (&r)[8], u4 (&o)[8]) {
#pragma unroll
  for (int c = 0; c < 4; ++c) {
#pragma unroll
    for (int kp = 0; kp < 4; ++kp) {
      unsigned a = r[2 * kp][c], b = r[2 * kp + 1][c];
      o[2 * c][kp] = __builtin_amdgcn_perm(b, a, 0x05040100);
      o[2 * c + 1][kp] = __builtin_amdgcn_perm(b, a, 0x07060302);
    }
  }
}
DEV void pre_load(int g, const unsigned short* wg, const unsigned short* wu, const unsigned short* wd, u4 (&r)[8]) {
  const int cls = __builtin_amdgcn_readfirstlane(g >= 2 * 15360 ? 2 : g >= 15360 ? 1 : 0);
  const char* w = (const char*)(cls == 2 ? wd : cls ? wu : wg);
  const int gg = g - cls * 15360;
  unsigned o0, o6, d1, d2;
  if (cls < 2) {
    const int no = gg % 192, ko = gg / 192;
    o0 = (ko * 8 * 1536 + no * 8) * 2; d1 = 3072; d2 = 6144; o6 = o0 + 6 * 3072;
  } else {
    const int no = gg % 80, kg = gg / 80;
    const int kap = kg >> 2, kq = kg & 3, nt = kap / 3, s = kap % 3;
    const int f0 = 96 * nt + 24 * kq;
    o0 = ((f0 + 4 * (s & 1) + 2 * (s >> 1)) * 640 + no * 8) * 2; d1 = 1280; d2 = 8 * 1280;
    o6 = ((f0 + 8 * (2 - s) + 6) * 640 + no * 8) * 2;
  }
#pragma unroll
  for (int j = 0; j < 8; ++j) r[j] = *(const u4*)(w + ((j < 6 ? o0 + (j >> 1) * d2 : o6) + (j & 1) * d1));
}
#define NOWT
constexpr int W2OFF = 262144;
DEV void pre_store(int g, const u4 (&r)[8], u4* W1t, u4* sm, int* tab, int lane) {
  u4 o[8];
  transpose8x8(r, o);
  const __amdgpu_buffer_rsrc_t rs = __builtin_amdgcn_make_buffer_rsrc((void*)W1t, 0, 6 << 20, 0x00020000);
  int b0, b1;
  if (g < 2 * 15360) {
    const int gu = g >= 15360;
    const int gg = g - gu * 15360;
    const int no = gg % 192, ko = gg / 192;
    const int nq = no / 3, i = no % 3, ks = ko >> 2, kq = ko & 3;
    b0 = ((nq * 20 + ks) * 3 + i) * 64 + kq * 16 + gu * 8;
    b1 = b0 + 4;
  } else {
    const int gg = g - 2 * 15360;
    const int no = gg % 80, kg = gg / 80;
    const int kap = kg >> 2, kq = kg & 3, nb = no / 10, ol = no % 10;
    const int n0 = 8 * ol, n1 = n0 + 4, bb = W2OFF + (nb * 48 + kap) * 320 + kq * 16;
    b0 = bb + ((n0 % 20) >> 2) * 64 + 4 * (n0 / 20);
    b1 = bb + ((n1 % 20) >> 2) * 64 + 4 * (n1 / 20);
  }
  tab[2 * lane] = b0; tab[2 * lane + 1] = b1;
#pragma unroll
  for (int c = 0; c < 8; ++c) sm[lane * 8 + (c ^ (lane & 7))] = o[c];
  __builtin_amdgcn_fence(__ATOMIC_RELEASE, "wavefront");
  __builtin_amdgcn_wave_barrier();
  __builtin_amdgcn_fence(__ATOMIC_ACQUIRE, "wavefront");
#pragma unroll
  for (int c = 0; c < 8; ++c) {
    const int src = c * 8 + (lane >> 3), pc = lane & 7;
    W1t[tab[2 * src + (pc >> 2)] + (pc & 3)] = sm[src * 8 + (pc ^ (src & 7))];
  }
}

constexpr int L1P = 170;
constexpr int L2P = 648;
constexpr int T2P = 2576;

#define WT1 16
#define WT2 16
#define ST1(p, v) __builtin_amdgcn_raw_buffer_store_b128(__builtin_bit_cast(u4, v), __builtin_amdgcn_make_buffer_rsrc((void*)T1, 0, 6 << 20, 0x00020000), (int)((char*)(p) - (char*)T1), 0, WT1)
template <int I, int N, class F> DEV void sfor(F&& f) {
  if constexpr (I < N) { f(std::integral_constant<int, I>{}); sfor<I + 1, N>(f); }
}
template <int OFF, class T> DEV void gld(T& d, int vo, const void* sb) {
  if constexpr (sizeof(T) == 16) asm volatile("global_load_dwordx4 %0, %1, %2 offset:%c3" : "=v"(d) : "v"(vo), "s"(sb), "n"(OFF) : "memory");
  else asm volatile("global_load_dword %0, %1, %2 offset:%c3" : "=v"(d) : "v"(vo), "s"(sb), "n"(OFF) : "memory");
}
template <int NWV> DEV unsigned own_code(const char* c0, const char* c1, int t) {
  unsigned v;
  const int vo = ((t >> 6) + NWV * (t & 63)) * 128;
  asm volatile("v_cmp_gt_u32 vcc, %3, %1\n s_and_saveexec_b64 s[42:43], vcc\n global_load_dword %0, %1, %2\n"
               "s_mov_b64 exec, s[42:43]" : "=v"(v) : "v"(vo), "s"(c0), "s"((int)(c1 - c0)) : "vcc", "s42", "s43", "scc", "memory");
  return v;
}
template <int OFF> DEV void hld(float& d, int vo, const void* sb) {
  d = 0.f;
  asm volatile("global_load_short_d16_hi %0, %1, %2 offset:%c3" : "+v"(d) : "v"(vo), "s"(sb), "n"(OFF) : "memory");
}
extern "C" __global__ __launch_bounds__(512) void k1_rowfwd(const unsigned* __restrict__ x, f4* __restrict__ T1,
                                                            const unsigned short* __restrict__ wr,
                                                            const unsigned short* __restrict__ wi, f4* Wt,
                                                            unsigned* __restrict__ cnt SARG) {
  __shared__ __attribute__((aligned(16))) float L0[8 * 640];
  __shared__ __attribute__((aligned(16))) f2 L1[16 * L1P];
  __shared__ __attribute__((aligned(16))) f2 L2[4 * L2P];
  const int b = blockIdx.x, t = threadIdx.x, lane = t & 63;
  const int w = __builtin_amdgcn_readfirstlane(t >> 6);
  const int s0 = (b & 7) * 32 + (b >> 3);
  STAMP_BEGIN();
  STAMP(0, 0);
  const unsigned* xp;
  asm volatile("s_load_dwordx2 %0, %1, 0x0" : "=s"(xp) : "s"(__builtin_amdgcn_kernarg_segment_ptr()) : "memory");
  const char *c0, *c1;
  SYM_ADDR(c0, k1_rowfwd);
  SYM_ADDR(c1, k2_col);
  const unsigned wa = own_code<8>(c0, c1, t);
  const int xo = (s0 + 256 * w) * 320;
  asm volatile("s_waitcnt lgkmcnt(0)" : "+s"(xp) : "s"(xo) : "memory");
  if (__builtin_expect(b >= 256, 0)) {
    const int rho = w & 3, s0 = (b - 256) * 2 + (w >> 2);
    unsigned w0;
    float ar[5], ai[5], br[5], bi[5], er, ei;
    {
      const int p0 = 8 * s0 + rho, p1 = 8 * s0 + (rho ? 8 - rho : 4);
      const int ia = (p0 * 321 + lane) * 2, ib = (p1 * 321 + (rho ? 320 - lane : lane)) * 2, st = rho ? -128 : 128;
      const int er0 = (lane & 1) ? p1 : p0, er1 = lane >= 2 ? (2048 - er0) & 2047 : er0;
      const int ie = 2 * (er1 * 321 + ((lane == 2 || lane == (rho ? 2 : 3)) ? 0 : 320));
      sfor<0, 5>([&](auto c) {
        constexpr int i = c.value;
        hld<128 * i>(ar[i], ia, wr); hld<128 * i>(ai[i], ia, wi);
        hld<0>(br[i], ib + st * i, wr); hld<0>(bi[i], ib + st * i, wi);
      });
      hld<0>(er, ie, wr); hld<0>(ei, ie, wi);
      const char* c2;
      SYM_ADDR(c2, k9_end);
      const int q = ((b - 256) >> 3) * 2 + (w >> 2);
      const char* pa = c1 + (q * 3 + lane % 3 + rho * 96) * 128;
      pa = pa < c2 ? pa : c1;
      w0 = touch(pa);
    }
    STAMP(0, 3);
    asm volatile("s_waitcnt vmcnt(1)" : "+v"(ar[0]), "+v"(ar[1]), "+v"(ar[2]), "+v"(ar[3]), "+v"(ar[4]), "+v"(ai[0]), "+v"(ai[1]),
                 "+v"(ai[2]), "+v"(ai[3]), "+v"(ai[4]), "+v"(br[0]), "+v"(br[1]), "+v"(br[2]), "+v"(br[3]), "+v"(br[4]),
                 "+v"(bi[0]), "+v"(bi[1]), "+v"(bi[2]), "+v"(bi[3]), "+v"(bi[4]), "+v"(er), "+v"(ei) : "v"(wa) : "memory");
    auto ex = [&](int j) {
      return f2{__int_as_float(__builtin_amdgcn_readlane(__float_as_int(er), j)),
                __int_as_float(__builtin_amdgcn_readlane(__float_as_int(ei), j))};
    };
    const float sc = (rho ? 2.f : 0.5f) / (2048.f * 640.f);
    const __amdgpu_buffer_rsrc_t rs = __builtin_amdgcn_make_buffer_rsrc((void*)Wt, 0, 161 * 32768, 0x00020000);
    const int off = ((rho * 40 + (rho > 0)) * 2048 + s0 * 8 + (lane >> 3) * 2048 + (lane & 7)) * 16;
#pragma unroll
    for (int i = 0; i < 6; ++i) {
      f2 a = i < 5 ? f2{ar[i % 5], ai[i % 5]} : ex(0), c = i < 5 ? f2{br[i % 5], bi[i % 5]} : ex(1);
      if (i == 0 || i == 5) {
        const f2 sa = ex(i ? 4 : 2), sc2 = ex(i ? 5 : 3);
        const f2 as = (a + f2{sa.x, -sa.y}) * 0.5f, cs = (c + f2{sc2.x, -sc2.y}) * 0.5f;
        if (lane == 0) { a = as; c = cs; }
      }
      a *= sc; c *= sc;
      const f4 o = {a.x, a.y, c.x, c.y};
      if (i < 5 || (rho == 0 && lane == 0))
        __builtin_amdgcn_raw_buffer_store_b128(__builtin_bit_cast(u4, o), rs, off + i * 8 * 32768, 0, 16);
    }
    if (b < 272 && t >= 448 && t < 484) cnt[((b - 256) * 36 + t - 448) * 32] = 0u;
    STAMP(0, 4);
    asm volatile("s_waitcnt vmcnt(0)" ::"v"(w0) : "memory");
    STAMP(0, 7);
    STAMP_END(0);
    return;
  }
  const unsigned* xrow = xp + xo;
  u4 xa;
  unsigned xb;
  gld<0>(xa, lane * 16, xrow);
  gld<1024>(xb, lane * 4, xrow);
  f2 ts1, ts2;
  asm volatile("s_load_dwordx2 %0, %2, %3\n s_load_dwordx2 %1, %2, %4" : "=&s"(ts1), "=&s"(ts2)
               : "s"(TS), "s"(s0 * 64 + (w & 3) * 8), "s"(s0 * 64 + ((8 - w) & 7) * 8) : "memory");
  f4 twd[2], twr[5];
  if (w < 4) {
    gld<0>(twd[0], (t < 160 ? t : 0) * 32, TWD);
    gld<16>(twd[1], (t < 160 ? t : 0) * 32, TWD);
    sfor<0, 5>([&](auto c) { gld<16 * c.value>(twr[c.value], (lane >> 2) * 80, TWR); });
    STAMP(0, 6);
    asm volatile("s_waitcnt vmcnt(7)" ::: "memory");
  } else {
    STAMP(0, 6);
    asm volatile("s_waitcnt vmcnt(0)" ::: "memory");
  }
  asm volatile("" : "+v"(xa), "+v"(xb) : "v"(wa) : "memory");
  STAMP(0, 1);
  {
    f2 xv[5] = {bf_up(xa.x), bf_up(xa.y), bf_up(xa.z), bf_up(xa.w), bf_up(xb)};
    unsigned o[5];
    ln_row(xv, o);
    float* p = L0 + w * 640;
    *(f4*)(p + 8 * lane) = f4{bf_lo(o[0]), bf_hi(o[0]), bf_lo(o[1]), bf_hi(o[1])};
    *(f4*)(p + 8 * lane + 4) = f4{bf_lo(o[2]), bf_hi(o[2]), bf_lo(o[3]), bf_hi(o[3])};
    *(f2*)(p + 512 + 2 * lane) = bf_up(o[4]);
  }
  STAMP(0, 5);
  __syncthreads();
  STAMP(0, 2);
  if (w >= 4) return;
  asm volatile("s_waitcnt vmcnt(5) lgkmcnt(0)" : "+v"(twd[0]), "+v"(twd[1]), "+s"(ts1), "+s"(ts2)::"memory");
  if (t < 160) {
    const f2 rr = {0.70710678118654752f, 0.70710678118654752f};
    f2 z[4][4];
#pragma unroll
    for (int q = 0; q < 4; ++q) {
      const float* p = L0 + t + 160 * q;
      f2 P = {p[0], p[2 * 640]}, Q = {p[4 * 640], p[6 * 640]};
      f2 S = {p[640], p[3 * 640]}, T = {p[5 * 640], p[7 * 640]};
      f2 E = P + Q, K = P - Q, G = S + T, H = S - T;
      f2 V = xx_mp_yy(H, H), M = xx_pm_yy(E, E), N = yy_pm_xx(G, G);
      z[0][q] = xx_pm_xx(M, N);
      z[1][q] = fma_pmc(V, rr, K);
      z[2][q] = f2{M.y, N.y};
      z[3][q] = fmsn(V, rr, K);
    }
    const f2 d1 = {twd[0].x, twd[0].y}, d2 = {twd[0].z, twd[0].w}, d3 = {twd[1].x, twd[1].y};
#pragma unroll
    for (int s = 0; s < 4; ++s) {
      dft4<-1>(z[s][0], z[s][1], z[s][2], z[s][3]);
      f2* p = L1 + s * 4 * L1P + t;
      p[0] = z[s][0];
      p[L1P] = cmul(z[s][1], d1);
      p[2 * L1P] = cmul(z[s][2], d2);
      p[3 * L1P] = cmul(z[s][3], d3);
    }
  }
  __syncthreads();
  STAMP(0, 3);
  if (t < 160) {
    const f2* p = L1 + (t / 10) * (L1P - 10) + t;
    f2 v[16];
#pragma unroll
    for (int a = 0; a < 16; ++a) v[a] = p[10 * a];
    dft16<-1>(v);
    f2* q = L2 + (t / 40) * (L2P - 40) + t;
#pragma unroll
    for (int a = 0; a < 16; ++a) q[40 * a] = v[a];
  }
  __syncthreads();
  STAMP(0, 4);
  asm volatile("s_waitcnt vmcnt(0)" : "+v"(twr[0]), "+v"(twr[1]), "+v"(twr[2]), "+v"(twr[3]), "+v"(twr[4])::"memory");
  {
    const f4* p = (const f4*)(L2 + w * L2P + 10 * lane);
    f2 v[10];
#pragma unroll
    for (int i = 0; i < 5; ++i) {
      f4 q = p[i];
      v[2 * i] = f2{q.x, q.y}; v[2 * i + 1] = f2{q.z, q.w};
    }
#pragma unroll
    for (int i = 1; i < 10; ++i) {
      const f4 q = twr[(i - 1) >> 1];
      v[i] = cmul(v[i], (i & 1) ? f2{q.x, q.y} : f2{q.z, q.w});
    }
    dft10<-1>(v);
    f4* dst = T1 + (size_t)((w * 40 + (w > 0)) * 2048 + s0 * 8) + ((lane >> 3) * 2048 + (lane & 7));
    if (w != 0) {
#pragma unroll
      for (int i = 0; i < 5; ++i) {
        f2 a = cmul<true>(v[i], ts1), c = cjmul<true>(v[i + 5], ts2);
        ST1(dst + i * 8 * 2048, (f4{a.x, a.y, c.x, c.y}));
      }
    } else {
      f2* S = (f2*)L0;
#pragma unroll
      for (int i = 5; i < 10; ++i) S[lane + 64 * (i - 5)] = v[i];
      if (lane == 0) S[320] = v[0];
      __builtin_amdgcn_fence(__ATOMIC_RELEASE, "wavefront");
      __builtin_amdgcn_wave_barrier();
      __builtin_amdgcn_fence(__ATOMIC_ACQUIRE, "wavefront");
#pragma unroll
      for (int i = 0; i < 5; ++i) {
        f2 P = S[320 - lane - 64 * i];
        f2 a = addc(v[i], P), c = cmul<true>(subc(v[i], P), ts1);
        ST1(dst + i * 8 * 2048, (f4{a.x, a.y, c.x, c.y}));
      }
      if (lane == 0) {
        f2 a = addc(v[5], v[5]), c = cmul<true>(subc(v[5], v[5]), ts1);
        dst[5 * 8 * 2048] = f4{a.x, a.y, c.x, c.y};
      }
    }
  }
  STAMP(0, 7);
  STAMP_END(0);
}

constexpr int EP = 272;
#define PD 0
extern "C" __global__ __launch_bounds__(256) void k2_col(const f2* __restrict__ T1, f2* __restrict__ T2,
                                                         const f2* __restrict__ Wt,
                                                         const unsigned short* __restrict__ wg,
                                                         const unsigned short* __restrict__ wu,
                                                         const unsigned short* __restrict__ wd, u4* __restrict__ W1t,
                                                         u4* __restrict__ W2t SARG) {
  extern __shared__ __attribute__((aligned(16))) char smem[];
  f2* E1 = (f2*)smem;
  f2* E2 = E1 + 16 * EP;
  const int tile = blockIdx.x, t = threadIdx.x;
  STAMP_BEGIN();
  STAMP(1, 0);
  const char *c0, *c1;
  SYM_ADDR(c0, k2_col);
  SYM_ADDR(c1, k3_mlp);
  const unsigned wa = own_code<4>(c0, c1, t);
  if (tile >= 161) {
    const int task = (tile - 161) * 256 + t, task2 = task + 24320 < 46080 ? task + 24320 : task;
    u4 r0[8], r1[8];
    __builtin_amdgcn_s_sleep(PD);
    asm volatile("" ::: "memory");
    pre_load(task, wg, wu, wd, r0);
    pre_load(task2, wg, wu, wd, r1);
    __builtin_amdgcn_sched_barrier(0);
    asm volatile("" ::"v"(wa), "v"(r0[0].x));
    u4* sm = (u4*)smem + (t >> 6) * 1088;
    pre_store(task, r0, W1t, sm, (int*)(sm + 1024), t & 63);
    pre_store(task2, r1, W1t, sm + 512, (int*)(sm + 1024) + 128 - 64 * 0, t & 63);
    STAMP(1, 9);
    STAMP_END(1);
    return;
  }
  const int i = t >> 4, kk = t & 15;
  f2 v[16], w[16];
  f4 tw4[8];
  {
    const f2* src = T1 + (size_t)tile * 4096 + t;
#pragma unroll
    for (int mm = 0; mm < 16; ++mm) v[mm] = src[mm * 256];
#pragma unroll
    for (int j = 0; j < 8; ++j) tw4[j] = *(const f4*)(TWC + i * 32 + 4 * j);
    const f2* wsrc = Wt + (size_t)tile * 4096 + t;
#pragma unroll
    for (int mm = 0; mm < 16; ++mm) w[mm] = wsrc[mm * 256];
  }
  STAMP(1, 1);
  dft16<-1>(v);
  asm volatile("" ::"v"(wa), "v"(v[0].x));
  E1[t] = v[0];
#pragma unroll
  for (int u = 1; u < 16; ++u) {
    const f4 q = tw4[u >> 1];
    E1[u * EP + t] = cmul(v[u], (u & 1) ? f2{q.z, q.w} : f2{q.x, q.y});
  }
  __syncthreads();
  STAMP(1, 2);
#pragma unroll
  for (int j = 0; j < 16; ++j) v[j] = E1[i * EP + j * 16 + kk];
  dft16<-1>(v);
  STAMP(1, 3);
#pragma unroll
  for (int j = 0; j < 16; ++j) v[j] = cmul(v[j], w[j]);
  dft16<1>(v);
  E2[t] = v[0];
#pragma unroll
  for (int u = 1; u < 16; ++u) {
    const f4 q = tw4[u >> 1];
    E2[u * EP + t] = cmulc(v[u], (u & 1) ? f2{q.z, q.w} : f2{q.x, q.y});
  }
  __syncthreads();
  STAMP(1, 4);
#pragma unroll
  for (int j = 0; j < 16; ++j) v[j] = E2[i * EP + j * 16 + kk];
  dft16<1>(v);
  {
    f2* dst = T2 + tile * 16 + (i * T2P + kk);
#pragma unroll
    for (int mm = 0; mm < 16; ++mm) {
      __builtin_amdgcn_raw_buffer_store_b64(__builtin_bit_cast(u2, v[mm]), __builtin_amdgcn_make_buffer_rsrc((void*)T2, 0, 6 << 20, 0x00020000),
                                            (tile * 16 + i * T2P + kk) * 8, mm * 16 * T2P * 8, WT2);
    }
  }
  STAMP(1, 7);
  STAMP_END(1);
}

constexpr int B_LDS = 163840;
typedef __attribute__((address_space(1))) unsigned gu32;
typedef __attribute__((address_space(3))) void* lds_t;
DEV f2 gelu2(f2 x) {
  const float k0 = -0.7978845608028654f * 2.f * 1.4426950408889634f, k1 = 0.044715f * k0;
  f2 z = x * (x * x * k1 + k0);
  f2 d = f2{__builtin_amdgcn_exp2f(z.x), __builtin_amdgcn_exp2f(z.y)} + 1.f;
  return x * f2{__builtin_amdgcn_rcpf(d.x), __builtin_amdgcn_rcpf(d.y)};
}
DEV unsigned geglu2(unsigned g, unsigned u) { return pack_bf2(rbf2(gelu2(bf_up(g))) * bf_up(u)); }
#define MFMA(a, b, c) __builtin_amdgcn_mfma_f32_16x16x32_bf16(__builtin_bit_cast(bf16x8, a), __builtin_bit_cast(bf16x8, b), c, 0, 0, 0)
#define WAITV(n) do { asm volatile("" ::: "memory"); __builtin_amdgcn_s_waitcnt(((n) & 15) | 0xF70 | (((n) >> 4) << 14)); asm volatile("" ::: "memory"); } while (0)
#define WAITL() do { asm volatile("" ::: "memory"); __builtin_amdgcn_s_waitcnt(0xC07F); asm volatile("" ::: "memory"); } while (0)
#define PIN(x) asm volatile("" : "+v"(x))
#define BAR() do { asm volatile("" ::: "memory"); __builtin_amdgcn_s_barrier(); asm volatile("" ::: "memory"); } while (0)
#define FENCE() asm volatile("" ::: "memory")
DEV unsigned flag_ld(gu32* c) { return __hip_atomic_load(c, __ATOMIC_RELAXED, __HIP_MEMORY_SCOPE_AGENT); }
DEV void flag_add(gu32* c, int lane) {
  if (lane == 0) __hip_atomic_fetch_add(c, 1u, __ATOMIC_RELAXED, __HIP_MEMORY_SCOPE_AGENT);
}
DEV void flag_wait(gu32* c, unsigned v) {
  v = __builtin_amdgcn_readfirstlane(v);
  for (int spin = 0; v < 16u && spin < (1 << 20); ++spin) {
    __builtin_amdgcn_s_sleep(1);
    v = __builtin_amdgcn_readfirstlane(flag_ld(c));
  }
  FENCE();
}
#define SB() __builtin_amdgcn_sched_barrier(0)
template <int R, bool Z> DEV void mf_av(f4& acc, const u4& b) {
  if (Z) asm volatile("v_mfma_f32_16x16x32_bf16 %0, a[%c2:%c3], %1, 0" : "+v"(acc) : "v"(b), "n"(R), "n"(R + 3));
  else asm volatile("v_mfma_f32_16x16x32_bf16 %0, a[%c2:%c3], %1, %0" : "+v"(acc) : "v"(b), "n"(R), "n"(R + 3));
}
template <int R, bool Z> DEV void mf_va(f4& acc, const u4& a) {
  if (Z) asm volatile("v_mfma_f32_16x16x32_bf16 %0, %1, a[%c2:%c3], 0" : "+v"(acc) : "v"(a), "n"(R), "n"(R + 3));
  else asm volatile("v_mfma_f32_16x16x32_bf16 %0, %1, a[%c2:%c3], %0" : "+v"(acc) : "v"(a), "n"(R), "n"(R + 3));
}
template <int RA, int RB> DEV void mf_aa(f4& acc) {
  asm volatile("v_mfma_f32_16x16x32_bf16 %0, a[%c1:%c2], a[%c3:%c4], %0" : "+v"(acc) : "n"(RA), "n"(RA + 3), "n"(RB), "n"(RB + 3));
}
template <bool Z> DEV void mf_vv(f4& acc, const u4& a, const u4& b) {
  if (Z) asm volatile("v_mfma_f32_16x16x32_bf16 %0, %1, %2, 0" : "+v"(acc) : "v"(a), "v"(b));
  else asm volatile("v_mfma_f32_16x16x32_bf16 %0, %1, %2, %0" : "+v"(acc) : "v"(a), "v"(b));
}
template <int R, int IDX, bool SC1> DEV void ld_a(const u4& rs, int l16, int so) {
  if (SC1) asm volatile("buffer_load_dwordx4 a[%c0:%c1], %2, %3, %4 offen offset:%c5 sc1"
                        :: "n"(R), "n"(R + 3), "v"(l16), "s"(rs), "s"(so + (IDX >> 2) * 4096), "n"((IDX & 3) * 1024) : "memory");
  else asm volatile("buffer_load_dwordx4 a[%c0:%c1], %2, %3, %4 offen offset:%c5"
                    :: "n"(R), "n"(R + 3), "v"(l16), "s"(rs), "s"(so + (IDX >> 2) * 4096), "n"((IDX & 3) * 1024) : "memory");
}
DEV u4 mk_rs(const void* p, unsigned bytes) {
  const unsigned long long a = (unsigned long long)p;
  return u4{(unsigned)a, (unsigned)(a >> 32), bytes, 0x00020000u};
}
#define V1(op, d) asm volatile(op " %0, %0" : "+v"(d))
#define V2(op, d, a, b) asm volatile(op " %0, %1, %2" : "=v"(d) : "v"(a), "v"(b))
struct Epi {
  float r[3][4], p[3][2];
  unsigned lo[3], y1, z1;
  u4 o;
  template <int I> DEV void cvt(const f4& a) {
#pragma unroll
    for (int e = 0; e < 4; ++e) asm volatile("v_cvt_pk_bf16_f32 %0, 0, %1" : "=v"(r[I][e]) : "v"(a[e]));
  }
  static constexpr int NS = 66;
  static constexpr int cost(int n) {
    const int s = n / 6;
    return n >= 63 ? 13 : n >= 60 ? 4 : s == 0 ? 9 : (s == 4 || s == 6) ? 8 : 4;
  }
  template <int n> DEV void step(float k1) {
    constexpr int s = n / 6, e = (n / 3) & 1, i = n % 3;
    if constexpr (n >= 63) {
      if (n == 63) z1 = __builtin_amdgcn_permlane16_swap(lo[0], lo[0], false, false)[1];
      if (n == 64) y1 = __builtin_amdgcn_permlane16_swap(lo[2], lo[1], false, false)[1];
      if (n == 65) o = u4{lo[0], lo[1], lo[2], __builtin_amdgcn_permlane32_swap(y1, z1, false, false)[1]};
    } else if constexpr (n >= 60) {
      lo[i] = pack_bf(r[i][0], r[i][1]); PIN(lo[i]);
    } else {
      if (s == 0) asm volatile("v_permlane32_swap_b32 %0, %1" : "+v"(r[i][e]), "+v"(r[i][2 + e]));
      if (s == 4) { p[i][e] = __builtin_amdgcn_exp2f(p[i][e]); PIN(p[i][e]); }
      if (s == 6) { p[i][e] = __builtin_amdgcn_rcpf(p[i][e]); PIN(p[i][e]); }
      if (s == 8) V1("v_cvt_pk_bf16_f32 %0, 0, %0 ;", r[i][e]);
      if (e == 0) {
        if (s == 1) p[i][e] = r[i][e] * r[i][e];
        if (s == 2) p[i][e] = __builtin_fmaf(k1, p[i][e], -0x1.26aec2p+1f);
        if (s == 3) p[i][e] = r[i][e] * p[i][e];
        if (s == 5) p[i][e] = p[i][e] + 1.f;
        if (s == 7) r[i][e] = r[i][e] * p[i][e];
        if (s == 9) r[i][e] = r[i][e] * r[i][2 + e];
        if (s == 1 || s == 2 || s == 3 || s == 5) PIN(p[i][e]);
        if (s == 7 || s == 9) PIN(r[i][e]);
      } else {
        if (s == 1) V2("v_mul_f32", p[i][e], r[i][e], r[i][e]);
        if (s == 2) asm volatile("v_fmaak_f32 %0, %1, %0, 0xc0135761" : "+v"(p[i][e]) : "v"(k1));
        if (s == 3) V2("v_mul_f32", p[i][e], r[i][e], p[i][e]);
        if (s == 5) V1("v_add_f32 %0, 1.0, %0 ;", p[i][e]);
        if (s == 7) V2("v_mul_f32", r[i][e], r[i][e], p[i][e]);
        if (s == 9) V2("v_mul_f32", r[i][e], r[i][e], r[i][2 + e]);
      }
    }
  }
  static constexpr int first(int g) {
    int tot = 0, acc = 0;
    for (int n = 0; n < NS; ++n) tot += cost(n);
    for (int n = 0; n < NS; ++n) {
      if (acc * 38 >= g * tot) return n;
      acc += cost(n);
    }
    return NS;
  }
  template <int c> DEV void gap(float k1) {
    if constexpr (c % 3 != 1) {
      constexpr int g = (c / 3 - 1) * 2 + (c % 3 == 2);
      sfor<first(g), first(g + 1)>([&](auto n) { step<n.value>(k1); });
    }
  }
};
template <int OFF> DEV void dsr(u4& d, int a) { asm volatile("ds_read_b128 %0, %1 offset:%c2" : "=v"(d) : "v"(a), "n"(OFF) : "memory"); }
template <int R, bool Z, int OFF> DEV void mf_av_ds(f4& acc, const u4& b, u4& d, int a) {
  if (Z) asm volatile("ds_read_b128 %1, %3 offset:%c6\n v_mfma_f32_16x16x32_bf16 %0, a[%c4:%c5], %2, 0"
                      : "+v"(acc), "=&v"(d) : "v"(b), "v"(a), "n"(R), "n"(R + 3), "n"(OFF) : "memory");
  else asm volatile("ds_read_b128 %1, %3 offset:%c6\n v_mfma_f32_16x16x32_bf16 %0, a[%c4:%c5], %2, %0"
                    : "+v"(acc), "=&v"(d) : "v"(b), "v"(a), "n"(R), "n"(R + 3), "n"(OFF) : "memory");
}
#define WAITLG(n) asm volatile("s_waitcnt lgkmcnt(%c0)" ::"n"(n) : "memory")
template <int R> DEV void flag_lda(gu32* c) {
  asm volatile("global_load_dword a%c0, %1, %2 sc1" ::"n"(R), "v"(0), "s"(c) : "memory");
}
template <int R> DEV unsigned flag_rda() {
  unsigned v;
  asm volatile("v_accvgpr_read_b32 %0, a%c1" : "=v"(v) : "n"(R));
  return v;
}

DEV void flag_wait_n(gu32* c, unsigned v, unsigned n) {
  v = __builtin_amdgcn_readfirstlane(v);
  for (int spin = 0; v < n && spin < (1 << 20); ++spin) {
    __builtin_amdgcn_s_sleep(1);
    v = __builtin_amdgcn_readfirstlane(flag_ld(c));
  }
  FENCE();
}
DEV void flag_chk(const u4& crs, int c, unsigned n) {
  unsigned v, s1, s2;
  asm volatile("v_accvgpr_read_b32 %0, a240\n s_nop 0\n v_readfirstlane_b32 %1, %0\n s_cmp_ge_u32 %1, %3\n s_cbranch_scc1 .Lfd%=\n"
               "s_mov_b32 %2, 0x100000\n"
               ".Lfl%=:\n s_sleep 1\n buffer_load_dword %0, off, %4, %5 sc1\n s_waitcnt vmcnt(0)\n v_readfirstlane_b32 %1, %0\n"
               "s_sub_u32 %2, %2, 1\n s_cbranch_scc1 .Lfd%=\n s_cmp_lt_u32 %1, %3\n s_cbranch_scc1 .Lfl%=\n .Lfd%=:"
               : "=&v"(v), "=&s"(s1), "=&s"(s2) : "s"(n), "s"(crs), "s"(c) : "memory", "scc");
}
DEV void flag_inc(const u4& crs, int c, int one, int l0) {
  asm volatile("buffer_atomic_add %0, %3, %1, %2 offen" ::"v"(one), "s"(crs), "s"(c), "v"(l0) : "memory");
}
template <int R> DEV void flag_get(const u4& crs, int c) {
  asm volatile("buffer_load_dword a%c0, off, %1, %2 sc1" ::"n"(R), "s"(crs), "s"(c) : "memory");
}
#define SPIN(x) asm volatile("" : "+s"(x))
template <int N> DEV void w2n(u4& d, const u4& rs, int l16) {
  asm volatile("buffer_load_dwordx4 %0, %1, %2, %3 offen offset:%c4" : "=v"(d)
               : "v"(l16), "s"(rs), "s"((N >> 2) * 4096), "n"((N & 3) * 1024) : "memory");
}
template <int N, class T> DEV void w1l(const u4& rs, int l16, T& tie) {
  asm volatile("buffer_load_dwordx4 a[%c1:%c2], %3, %4, %5 offen offset:%c6" : "+v"(tie)
               : "n"(4 * N), "n"(4 * N + 3), "v"(l16), "s"(rs), "s"((N >> 2) * 4096), "n"((N & 3) * 1024) : "memory");
}
template <int N, class T> DEV void w2l(u4& d, const u4& rs, int l16, T& tie) {
  asm volatile("buffer_load_dwordx4 %0, %2, %3, %4 offen offset:%c5" : "=v"(d), "+v"(tie)
               : "v"(l16), "s"(rs), "s"((N >> 2) * 4096), "n"((N & 3) * 1024) : "memory");
}
DEV void set_m0(int lds) { asm volatile("s_mov_b32 m0, %0" ::"s"(lds) : "memory"); }
template <int OFF> DEV void dma(const u4& rs, int l16, int so) {
  asm volatile("buffer_load_dwordx4 %0, %1, %2 offen offset:%c3 sc1 lds" ::"v"(l16), "s"(rs), "s"(so), "n"(OFF) : "memory");
}
template <int OFF, class T> DEV void bld_sc1(T& d, const u4& rs, int vo) {
  if constexpr (sizeof(T) == 16) asm volatile("buffer_load_dwordx4 %0, %1, %2, 0 offen offset:%c3 sc1" : "=v"(d) : "v"(vo), "s"(rs), "n"(OFF) : "memory");
  else asm volatile("buffer_load_dwordx2 %0, %1, %2, 0 offen offset:%c3 sc1" : "=v"(d) : "v"(vo), "s"(rs), "n"(OFF) : "memory");
}
constexpr int tL(int n) { return n < 10 ? n - 20 : (n / 5 - 2) * 100 + ((n / 5) & 1 ? 4 : 10) + 6 * (n % 5); }
constexpr int tE(int n) { return (n / 10) * 100 + (n % 10 < 6 ? 13 + 6 * (n % 10) : 46 + 3 * (n % 10 - 6)); }
constexpr int tA(int n) { return 438 + (n / 6) * 100 + 4 * (n % 6); }
constexpr int tB(int n) { return n < 7 ? 632 + 4 * n : 702 + 4 * (n - 7); }
constexpr int tC(int n) { return 921 + 3 * n; }
constexpr int tD(int n) { return n < 10 ? 1041 + 2 * n : 1101 + 2 * (n - 10); }
constexpr int tW2(int n) { return n < 13 ? 704 + 4 * n : n < 17 ? 810 + 15 * (n - 13) : 901 + 4 * (n - 17); }
constexpr int tS(int s) { return s < 7 ? 199 + 100 * s : 890; }
constexpr int tP(int p) { return p < 2 ? 341 + 200 * p : p == 2 ? 742 : 935; }
constexpr int tF(int n) { return n < 2 ? 134 + 200 * n : n == 2 ? 408 : n == 3 ? 608 : n == 4 ? 891 : 1012; }
constexpr int tK(int n) { return n < 2 ? 207 + 200 * n : n == 2 ? 437 : n == 3 ? 630 : n == 4 ? 920 : 1040; }
constexpr int tH(int n) { return n < 4 ? 39 + 6 * n : 1110 + 4 * (n - 4); }
constexpr int cnt_in(int (*f)(int), int n, int t0, int t1) {
  int c = 0;
  for (int i = 0; i < n; ++i) c += f(i) > t0 && f(i) < t1;
  return c;
}
constexpr int younger(int t0, int t1) {
  return cnt_in(tL, 40, t0, t1) + cnt_in(tE, 40, t0, t1) + cnt_in(tA, 12, t0, t1) + cnt_in(tW2, 20, t0, t1) + cnt_in(tB, 12, t0, t1) +
         cnt_in(tC, 12, t0, t1) + cnt_in(tD, 12, t0, t1) + cnt_in(tS, 8, t0, t1) + cnt_in(tP, 4, t0, t1) + cnt_in(tF, 6, t0, t1) +
         cnt_in(tH, 7, t0, t1);
}
constexpr int find(int (*f)(int), int n, int t) {
  for (int i = 0; i < n; ++i) if (f(i) == t) return i;
  return -1;
}

#define K3F 8192
#define NI 10
#define NA 8
#define NL 8
#define NW0 (NI + NA)
#define NP2 12
#define NPOST (46 - NW0 - NL - NP2)
extern "C" __global__ __launch_bounds__(256) __attribute__((amdgpu_waves_per_eu(1, 1)))
void k3_mlp(const f4* __restrict__ T2, const unsigned* __restrict__ x, const u4* __restrict__ W1t,
            const u4* __restrict__ W2t, unsigned* hbuf, u4* ln2T, u4* actT, unsigned* __restrict__ out,
            unsigned* cntp SARG) {
  extern __shared__ __attribute__((aligned(16))) char smem[];
  float* L0 = (float*)smem;
  f2* L1 = (f2*)(smem + 20480);
  f2* L2 = (f2*)(smem + 20480 + 16 * L1P * 8);
  u4* LS = (u4*)(smem + 65536);
  const int b = blockIdx.x, t = threadIdx.x, lane = t & 63;
  const int w = __builtin_amdgcn_readfirstlane(t >> 6);
  const int xcd = b & 7, idx = b >> 3;
  const int mt = (xcd >> 2) * 8 + (idx >> 2), nt = (xcd & 3) * 4 + (idx & 3);
  const int rs = idx & 1, nb = nt >> 1;
  const int s0 = mt * 16 + nt, q4 = nt >> 2;
  gu32* cnt = (gu32*)cntp;
  STAMP_BEGIN();
  STAMP(2, 0);
  asm volatile("" ::: "a255");
  const char *c0, *c1;
  SYM_ADDR(c0, k3_mlp);
  SYM_ADDR(c1, k9_end);
  unsigned tch = own_code<4>(c0, c0 + K3F, t), tch2;
  const u4 w1r = mk_rs(W1t + (size_t)(nt * 4 + w) * 3840, 61440);
  const u4 w2s = mk_rs(W2t + (size_t)(nb * 48 + w * 12) * 320, 61440);
  const int l16 = lane * 16;
  const int tt = t < 160 ? t : t - 32;
  f4 twd[2];
  u4 xa[2], w2[8][5];
  unsigned xb[2];
  {
    const char* src = (const char*)(T2 + (size_t)s0 * (T2P / 2) + (w * 320 + (w > 0) * 8));
    const f2* tsp = (const f2*)TS + s0 * 8;
    f4 q[5], m[5], twr[5];
    f2 v[10];
    sfor<0, 5>([&](auto i) { gld<i.value * 1024 - 2048>(q[i.value], l16, src + 2048); });
    asm volatile("s_cmp_eq_u32 %7, 0\n s_cselect_b64 exec, -1, 0\n global_load_dwordx4 %0, %5, %6 offset:2048\n global_load_dwordx4 %1, %5, %6 offset:1024\n"
                 "global_load_dwordx4 %2, %5, %6\n global_load_dwordx4 %3, %5, %6 offset:-1024\n"
                 "global_load_dwordx4 %4, %5, %6 offset:-2048\n s_mov_b64 exec, -1"
                 : "=&v"(m[0]), "=&v"(m[1]), "=&v"(m[2]), "=&v"(m[3]), "=&v"(m[4])
                 : "v"(1008 - l16), "s"(src + 257 * 16 - 2048), "s"(w) : "memory", "scc");
    sfor<0, 5>([&](auto i) { gld<i.value * 16>(twr[i.value], (lane >> 2) * 80, TWR); });
    gld<0>(twd[0], (tt < 160 ? tt : 0) * 32, TWD);
    gld<16>(twd[1], (tt < 160 ? tt : 0) * 32, TWD);
#pragma unroll
    for (int r = 0; r < 2; ++r) {
      const unsigned* xrow = x + (s0 + 256 * (w + 4 * r)) * 320;
      gld<0>(xa[r], l16, xrow);
      gld<1024>(xb[r], lane * 4, xrow);
    }
    tch2 = own_code<4>(c0 + K3F, c1, t);
    const f2 ts1 = tsp[w], ts2 = tsp[(8 - w) & 7];
    sfor<0, NI>([&](auto c) { ld_a<4 * c.value, c.value, false>(w1r, l16, 0); });
    asm volatile("s_waitcnt vmcnt(%c11)" : "+v"(q[0]), "+v"(q[1]), "+v"(q[2]), "+v"(q[3]), "+v"(q[4]), "+v"(m[0]), "+v"(m[1]),
                 "+v"(m[2]), "+v"(m[3]), "+v"(m[4]), "+v"(tch) : "n"(NI + 1) : "memory");
    asm volatile("" : "+v"(twr[0]), "+v"(twr[1]), "+v"(twr[2]), "+v"(twr[3]), "+v"(twr[4]), "+v"(twd[0]), "+v"(twd[1]),
                 "+v"(xa[0]), "+v"(xa[1]), "+v"(xb[0]), "+v"(xb[1]));
    STAMP(2, 1);
    if (w != 0) {
#pragma unroll
      for (int i = 0; i < 5; ++i) {
        v[i] = cmulc<true>(f2{q[i].x, q[i].y}, ts1);
        v[i + 5] = cjmul<true>(f2{q[i].z, q[i].w}, ts2);
      }
    } else {
#pragma unroll
      for (int i = 0; i < 5; ++i) {
        const f2 A = {q[i].x, q[i].y}, B = {q[i].z, q[i].w}, C = {m[i].x, m[i].y}, D = {m[i].z, m[i].w};
        v[i] = m2c<true>(B, ts1, f1cs(B, ts1, A));
        v[i + 5] = m2<true>(D, ts1, f1ns(D, ts1, C));
      }
    }
    sfor<0, (NA > 5 ? NA - 5 : 0)>([&](auto c) { if constexpr (c.value < 2) w1l<NI + c.value>(w1r, l16, v[4 + 5 * c.value]); });
    dft10<1>(v);
    sfor<2, (NA > 7 ? NA - 5 : 2)>([&](auto c) { w1l<NI + c.value>(w1r, l16, v[c.value]); });
    f4* p = (f4*)(L2 + w * L2P + 10 * lane);
    sfor<0, 5>([&](auto ic) {
      constexpr int i = ic.value;
      if constexpr (i > 0) { const f4 z = twr[i - 1]; v[2 * i] = cmulc(v[2 * i], f2{z.z, z.w}); }
      const f4 z = twr[i];
      v[2 * i + 1] = cmulc(v[2 * i + 1], f2{z.x, z.y});
      if constexpr (NA - 5 + i >= 0) w1l<NI + NA - 5 + i>(w1r, l16, v[2 * i + 1]);
      p[i] = f4{v[2 * i].x, v[2 * i].y, v[2 * i + 1].x, v[2 * i + 1].y};
    });
  }
  WAITL();
  BAR();
  if (w < 3) {
    const f2* q = L2 + (tt / 40) * (L2P - 40) + tt;
    f2 v[16];
#pragma unroll
    for (int a = 0; a < 16; ++a) v[(a >> 2) + 4 * (a & 3)] = q[40 * ((a >> 2) + 4 * (a & 3))];
    sfor<0, 4>([&](auto n) {
      dft4<1>(v[n.value], v[4 + n.value], v[8 + n.value], v[12 + n.value]);
      if constexpr (n.value & 1) w1l<NW0 + (n.value >> 1)>(w1r, l16, v[12 + n.value]);
    });
    dft16<1, false>(v);
    f2* p = L1 + (tt / 10) * (L1P - 10) + tt;
    sfor<0, 16>([&](auto ac) {
      constexpr int a = (ac.value >> 2) + 4 * (ac.value & 3);
      if constexpr ((ac.value & 3) == 0) w1l<NW0 + 2 + (ac.value >> 2)>(w1r, l16, v[a]);
      p[10 * a] = v[a];
    });
  } else {
    sfor<NW0, NW0 + 6>([&](auto c) { ld_a<4 * c.value, c.value, false>(w1r, l16, 0); });
    sfor<60 - NP2, 60>([&](auto c) { ld_a<4 * c.value, c.value, false>(w1r, l16, 0); });
  }
  WAITL();
  BAR();
  if (w < 3) {
    const f2 rr = {0.70710678118654752f, 0.70710678118654752f};
    const f2 d1 = {twd[0].x, twd[0].y}, d2 = {twd[0].z, twd[0].w}, d3 = {twd[1].x, twd[1].y};
    f2 z[4][4];
#pragma unroll
    for (int s = 0; s < 4; ++s) {
      const f2* p = L1 + s * 4 * L1P + tt;
      z[s][0] = p[0]; z[s][1] = p[L1P]; z[s][2] = p[2 * L1P]; z[s][3] = p[3 * L1P];
    }
    sfor<0, 4>([&](auto sc) {
      constexpr int s = sc.value;
      z[s][1] = cmulc(z[s][1], d1);
      z[s][2] = cmulc(z[s][2], d2);
      z[s][3] = cmulc(z[s][3], d3);
      dft4<1>(z[s][0], z[s][1], z[s][2], z[s][3]);
      w1l<NW0 + 6 + s>(w1r, l16, z[s][3]);
    });
    sfor<0, 4>([&](auto qc) {
      constexpr int q = qc.value;
      f2 M = xx_pm_yy(z[0][q], z[0][q]);
      f2 B02 = xx_pm_xx(M, z[2][q]), B13 = yy_mp_yy(M, z[2][q]);
      f2 O = addc(z[3][q], z[1][q]), D = subc(z[1][q], z[3][q]);
      f2 V = xx_mp_yy(D, D);
      w1l<NW0 + 10 + q>(w1r, l16, V);
      f2 Y02 = B02 + O, Y46 = B02 - O, Y13 = fma_pm(V, rr, B13), Y57 = fma_mp(V, rr, B13);
      float* p = L0 + tt + 160 * q;
      p[0] = Y02.x; p[2 * 640] = Y02.y;
      p[4 * 640] = Y46.x;
      p[6 * 640] = Y46.y;
      p[640] = Y13.x; p[3 * 640] = Y13.y;
      p[5 * 640] = Y57.x; p[7 * 640] = Y57.y;
    });
  } else {
    sfor<NW0 + 6, NW0 + 14>([&](auto c) { ld_a<4 * c.value, c.value, false>(w1r, l16, 0); });
  }
  WAITL();
  BAR();
  STAMP(2, 2);
  const __amdgpu_buffer_rsrc_t hrs = __builtin_amdgcn_make_buffer_rsrc((void*)hbuf, 0, 2048 * 1280, 0x00020000);
  const __amdgpu_buffer_rsrc_t lrs = __builtin_amdgcn_make_buffer_rsrc((void*)ln2T, 0, 2048 * 1280, 0x00020000);
  const __amdgpu_buffer_rsrc_t ars = __builtin_amdgcn_make_buffer_rsrc((void*)actT, 0, 16 * 9 * 49152, 0x00020000);
  unsigned hk[2][5];
  sfor<0, 2>([&](auto rc) {
    constexpr int r = rc.value;
    const int j = w + 4 * r;
    const float* p = L0 + j * 640;
    const f4 y0 = *(const f4*)(p + 8 * lane), y1 = *(const f4*)(p + 8 * lane + 4);
    const f2 y2 = *(const f2*)(p + 512 + 2 * lane);
    unsigned (&h)[5] = hk[r];
    unsigned o[5];
    h[0] = pack_bf2(bf_up(xa[r].x) + rbf2(f2{y0.x, y0.y}));
    h[1] = pack_bf2(bf_up(xa[r].y) + rbf2(f2{y0.z, y0.w}));
    h[2] = pack_bf2(bf_up(xa[r].z) + rbf2(f2{y1.x, y1.y}));
    h[3] = pack_bf2(bf_up(xa[r].w) + rbf2(f2{y1.z, y1.w}));
    h[4] = pack_bf2(bf_up(xb[r]) + rbf2(y2));
    if constexpr (NL > r) w1l<NW0 + 14 + r>(w1r, l16, h[0]);
    if constexpr (NL > 2 + r) w1l<NW0 + 16 + r>(w1r, l16, h[4]);
    f2 hv[5] = {bf_up(h[0]), bf_up(h[1]), bf_up(h[2]), bf_up(h[3]), bf_up(h[4])};
    ln_row(hv, o);
    if constexpr (NL > 4 + r) w1l<NW0 + 18 + r>(w1r, l16, o[0]);
    if constexpr (NL > 6 + r) w1l<NW0 + 20 + r>(w1r, l16, o[4]);
    LS[lane * 8 + (j ^ (lane & 7))] = u4{o[0], o[1], o[2], o[3]};
    ((unsigned*)LS)[((64 + (lane >> 2)) * 8 + (j ^ ((lane >> 2) & 7))) * 4 + (lane & 3)] = o[4];
  });
  WAITL();
  BAR();
  {
    const int base = ((s0 >> 1) * 1280 + (s0 & 1) * 8) * 16;
#pragma unroll
    for (int r = 0; r < 3; ++r) {
      const int p = t + 256 * r;
      if (r < 2 || t < 128) {
        const u4 v = LS[p];
        __builtin_amdgcn_raw_buffer_store_b128(v, lrs, base + (p >> 3) * 256 + ((p ^ (p >> 3)) & 7) * 16, 0, 16);
      }
    }
  }
  sfor<NW0 + 14 + NL, 60 - NP2>([&](auto c) { ld_a<4 * c.value, c.value, false>(w1r, l16, 0); });
  WAITV(NPOST);
  asm volatile("" ::"v"(tch2) : "memory");
  BAR();
  STAMP(2, 3);
  gu32* cl = cnt + mt * 128;
  if (w == 3) {
    flag_add(cl + q4 * 32, lane);
    flag_wait_n(cl + q4 * 32, flag_ld(cl + q4 * 32), 4u);
  } else {
    sfor<60 - NP2, 60>([&](auto c) { ld_a<4 * c.value, c.value, false>(w1r, l16, 0); });
  }
  BAR();
  STAMP(2, 4);
  const u4 lns = mk_rs(ln2T, 2048 * 1280), acs = mk_rs(actT, 16 * 9 * 49152), hs = mk_rs(hbuf, 2048 * 1280);
  const u4 crs = mk_rs(cntp, 1 << 16);
  const int lds0 = (int)(size_t)(lds_t)smem;
  const int lso = (mt * 8 * 1280 + w * 5 * 64) * 16;
  const int clo = mt * 512, cao = (64 + mt * 20 + 4) * 128;
  const int dld = lds0 + w * 5120;
  int one = 1, l0 = lane ? 0x40000000 : 0;
  float k1 = 0.044715f * (-0.7978845608028654f * 2.f * 1.4426950408889634f);
  PIN(one); PIN(k1);
  auto lfo = [&](int p) { return clo + ((q4 + p) & 3) * 128; };
  flag_get<240>(crs, lfo(1));
  sfor<0, 10>([&](auto n) {
    constexpr int o = (n.value / 5) * 20480 + (n.value % 5) * 1024;
    set_m0(dld + o);
    asm volatile("s_nop 0");
    dma<0>(lns, l16, lso + q4 * 40960 + o);
  });
  const int aoff = lane < 48 ? (((mt * 9) * 48 + nt * 3 + (lane >> 4)) * 64 + w * 16 + (lane & 15)) * 16 : 0x40000000;
  auto ast = [&](const u4& o, int so) { __builtin_amdgcn_raw_buffer_store_b128(o, ars, aoff, so, 16); };
  auto pub = [&](int pair) { return cao + (pair * 4 + q4) * 128; };
  auto sub = [&](int j) { return cao + (j * 4 + w) * 128; };
  auto bso = [&](int j) { return ((mt * 9 + 1 + 2 * j + rs) * 48 + w * 12) * 1024; };
  int dso[4], la[4];
#pragma unroll
  for (int p = 1; p < 4; ++p) { dso[p] = lso + ((q4 + p) & 3) * 40960; SPIN(dso[p]); }
#pragma unroll
  for (int p = 0; p < 4; ++p) { la[p] = lds0 + l16 + p * 40960; PIN(la[p]); }
  const int la2 = la[0] + w * 12288, l4 = lane * 4;
  u4 af[8];
  f4 acc[3];
#pragma unroll
  for (int i = 0; i < 3; ++i) acc[i] = f4{0.f, 0.f, 0.f, 0.f};
  WAITV(5);
  BAR();
  STAMP(2, 5);
  sfor<0, 6>([&](auto k) { dsr<k.value * 1024>(af[k.value], la[0]); });
  sfor<0, 8>([&](auto tc) {
    constexpr int TI = tc.value, F0 = (TI & 1) * 20;
    Epi ep;
    SB();
    sfor<0, 60>([&](auto cc) {
      constexpr int c = cc.value, k = c / 3, i = c % 3, T = TI * 100 + c;
      if constexpr (TI > 0) {
        if constexpr (k == 0) ep.template cvt<i>(acc[i]);
        else ep.template gap<c>(k1);
      }
      constexpr int nk = find(tK, 4, T);
      if constexpr (T == 7) flag_chk(crs, lfo(1), 4u);
      if constexpr (nk >= 0) { WAITV(younger(tF(nk), T)); flag_chk(crs, nk < 2 ? lfo(nk + 2) : sub(nk - 2), nk < 2 ? 4u : 16u); }
      if constexpr (T == 41) WAITV(younger(tL(9), T));
      if constexpr (T == 141) WAITV(younger(tL(14), T));
      if constexpr (find(tP, 3, T) >= 0) { WAITV(younger(tS(TI - 2), T)); flag_inc(crs, pub(find(tP, 3, T)), one, l0); }
      if constexpr (c == 41 && TI < 7) BAR();
      constexpr int nl = find(tL, 40, T), nl1 = find(tL, 40, T + 1), na = find(tA, 12, T), na1 = find(tA, 12, T + 1);
      constexpr int nb1 = find(tB, 12, T + 1), nbb = find(tB, 12, T);
      if constexpr (nl1 >= 0 && (nl1 % 5) % 4 == 0) set_m0(dld + (nl1 / 5) * 20480 + (nl1 % 5) * 1024);
      if constexpr (nl >= 0) dma<((nl % 5) & 3) * 1024>(lns, l16, dso[nl / 10] + ((nl / 5) & 1) * 20480 + ((nl % 5) >> 2) * 4096);
      if constexpr (na1 >= 0 && (na1 % 4 == 0 || na1 == 6)) set_m0(lds0 + w * 12288 + (na1 & ~3) * 1024);
      if constexpr (na >= 0) dma<(na & 3) * 1024>(acs, l16, bso(0) + (na >> 2) * 4096);
      if constexpr (nb1 >= 0 && nb1 % 4 == 0) set_m0(lds0 + 49152 + w * 12288 + nb1 * 1024);
      if constexpr (nbb >= 0) dma<(nbb & 3) * 1024>(acs, l16, bso(1) + (nbb >> 2) * 4096);
      constexpr int nf = find(tF, 4, T), ne = find(tE, 40, T), nw = find(tW2, 20, T), nh = find(tH, 4, T);
      if constexpr (nf >= 0) flag_get<240>(crs, nf < 2 ? lfo(nf + 2) : sub(nf - 2));
      if constexpr (ne >= 0) w2n<ne>(w2[ne / 5][ne % 5], w2s, l16);
      if constexpr (nw >= 0) ld_a<4 * nw, 40 + nw, false>(w2s, l16, 0);
      if constexpr (nh >= 0) {
        const unsigned (&h)[5] = hk[nh >> 1];
        const int ho = (s0 * 8 + w + 4 * (nh >> 1)) * 1280;
        if constexpr (nh & 1) __builtin_amdgcn_raw_buffer_store_b32(h[4], hrs, l4, ho + 1024, 16);
        else __builtin_amdgcn_raw_buffer_store_b128(u4{h[0], h[1], h[2], h[3]}, hrs, l16, ho, 16);
      }
      if constexpr (c % 12 == 0) WAITLG(2);
      SB();
      constexpr bool NX = TI == 7 && k >= 14;
      if constexpr (i == 1) mf_av_ds<c * 4, k == 0, (NX ? k - 14 : F0 + k + 6) * 1024>(acc[i], af[(F0 + k) % 8], af[(F0 + k + 6) % 8], NX ? la2 : la[TI / 2]);
      else mf_av<c * 4, k == 0>(acc[i], af[(F0 + k) % 8]);
      SB();
    });
    if constexpr (TI > 0) ast(ep.o, TI * 49152);
    asm volatile("s_nop 1" : "+v"(acc[0]), "+v"(acc[1]), "+v"(acc[2]));
    if constexpr (TI == 0) STAMP(2, 13);
    if constexpr (TI == 1) STAMP(2, 14);
    if constexpr (TI == 5) STAMP(2, 6);
  });
  STAMP(2, 7);
  asm volatile("s_nop 7" : "+v"(acc[0]), "+v"(acc[1]), "+v"(acc[2]));
  {
    Epi ep;
    sfor<0, 3>([&](auto c) { ep.template cvt<c.value>(acc[c.value]); });
    sfor<0, Epi::NS>([&](auto c) {
      if constexpr (find(tW2, 20, 800 + c.value) >= 0)
        ld_a<4 * find(tW2, 20, 800 + c.value), 40 + find(tW2, 20, 800 + c.value), false>(w2s, l16, 0);
      if constexpr (c.value >= 63) asm volatile("s_nop 1");
      ep.template step<c.value>(k1);
    });
    ast(ep.o, 8 * 49152);
  }
  flag_get<240>(crs, sub(2));
  STAMP(2, 8);
  const int vrow = (mt * 8 + ((w + 3) & 3) * 2 + rs) * 16 + (lane & 15);
  const int hoff = vrow * 1280 + nb * 160 + (lane >> 4) * 40;
  const int lr = la[0] + 98304 + ((w + 3) & 3) * 15360;
  u4 h0, h1, sm[5];
  u2 h2;
  f4* red = (f4*)(smem + 98304);
  f4 ac[5];
#pragma unroll
  for (int i = 0; i < 5; ++i) ac[i] = f4{0.f, 0.f, 0.f, 0.f};
  sfor<0, 4>([&](auto jc) {
    constexpr int j = jc.value;
    SB();
    sfor<0, 60>([&](auto cc) {
      constexpr int c = cc.value, k = c / 5, i = c % 5, T = 900 + 100 * j + c, g = 12 * j + k;
      if constexpr (T == tP(3)) { WAITV(younger(tS(7), T)); flag_inc(crs, pub(3), one, l0); }
      if constexpr (T == tF(5)) flag_get<240>(crs, sub(3));
      if constexpr (T == 940) WAITV(younger(tW2(19), T));
      if constexpr (T == tK(4)) { WAITV(younger(tF(4), T)); flag_chk(crs, sub(2), 16u); }
      if constexpr (T == tK(5)) { WAITV(younger(tF(5), T)); flag_chk(crs, sub(3), 16u); }
      if constexpr (j == 3 && i == 0) WAITV(younger(tD(k), T));
      if constexpr (find(tW2, 20, T) >= 0) ld_a<4 * find(tW2, 20, T), 40 + find(tW2, 20, T), false>(w2s, l16, 0);
      if constexpr (find(tC, 12, T) >= 0) ld_a<80 + 4 * find(tC, 12, T), find(tC, 12, T), true>(acs, l16, bso(2));
      if constexpr (find(tD, 12, T) >= 0) ld_a<128 + 4 * find(tD, 12, T), find(tD, 12, T), true>(acs, l16, bso(3));
      if constexpr (T == tH(4)) bld_sc1<0>(h0, hs, hoff);
      if constexpr (T == tH(5)) bld_sc1<16>(h1, hs, hoff);
      if constexpr (T == tH(6)) bld_sc1<32>(h2, hs, hoff);
      if constexpr (j < 2 && i == 2 && g < 18) dsr<(g + 6) * 1024 + (g >= 6 ? 36864 : 0)>(af[(g + 6) % 8], la2);
      if constexpr (j < 2 && c % 20 == 0) WAITLG(g == 12 ? 7 : g == 20 ? 0 : 2);
      if constexpr (j == 3) {
        constexpr int ri = c < 16 ? c / 4 : (c - 28) / 4 + 4, rsx = c % 4;
        if constexpr ((c < 16 || (c >= 28 && c < 32)) && rsx < 3) dsr<(rsx * 5 + ri) * 1024>(rsx ? af[(2 * ri + rsx - 1) % 8] : sm[ri], lr);
        if constexpr (c == 24 || c == 44) WAITLG(0);
        if constexpr ((c >= 24 && c < 40) || (c >= 44 && c < 48)) {
          constexpr int ai = c < 40 ? (c - 24) / 4 : 4, as = (c >> 1) & 1, e = (c & 1) * 2;
          asm volatile("v_add_f32 %0, %0, %2\n v_add_f32 %1, %1, %3" : "+v"(sm[ai][e]), "+v"(sm[ai][e + 1])
                       : "v"(af[(2 * ai + as) % 8][e]), "v"(af[(2 * ai + as) % 8][e + 1]));
        }
      }
      SB();
      if constexpr (j < 2) {
        if constexpr (k < 8) mf_vv<k == 0>(ac[i], w2[k][i], af[g % 8]);
        else mf_av<(c - 40) * 4, false>(ac[i], af[g % 8]);
      } else {
        if constexpr (k < 8) mf_va<j * 48 - 16 + k * 4, k == 0>(ac[i], w2[k][i]);
        else mf_aa<(c - 40) * 4, j * 48 - 16 + k * 4>(ac[i]);
      }
      SB();
    });
    asm volatile("s_nop 7" : "+v"(ac[0]), "+v"(ac[1]), "+v"(ac[2]), "+v"(ac[3]), "+v"(ac[4]));
    if constexpr (j == 0) BAR();
    if constexpr (j < 3) {
      f4* p = red + ((((w + j) & 3) * 3 + j) * 5) * 64 + lane;
#pragma unroll
      for (int i = 0; i < 5; ++i) p[i * 64] = ac[i];
      asm volatile("s_nop 1");
    }
    if constexpr (j == 2) { WAITL(); BAR(); }
    STAMP(2, 9 + j);
  });
#pragma unroll
  for (int i = 0; i < 5; ++i) ac[i] += __builtin_bit_cast(f4, sm[i]);
  asm volatile("s_waitcnt vmcnt(0)" : "+v"(h0), "+v"(h1), "+v"(h2)::"memory");
  const unsigned hh[10] = {h0.x, h0.y, h0.z, h0.w, h1.x, h1.y, h1.z, h1.w, h2.x, h2.y};
  unsigned r[10];
#pragma unroll
  for (int i = 0; i < 5; ++i) {
    r[2 * i] = pack_bf2(bf_up(hh[2 * i]) + rbf2(f2{ac[i][0], ac[i][1]}));
    r[2 * i + 1] = pack_bf2(bf_up(hh[2 * i + 1]) + rbf2(f2{ac[i][2], ac[i][3]}));
  }
  const int row = (vrow >> 3) + 256 * (vrow & 7);
  unsigned* op = out + row * 320 + nb * 40 + (lane >> 4) * 10;
  *(u2*)(op) = u2{r[0], r[1]};
  *(u2*)(op + 2) = u2{r[2], r[3]};
  *(u2*)(op + 4) = u2{r[4], r[5]};
  *(u2*)(op + 6) = u2{r[6], r[7]};
  *(u2*)(op + 8) = u2{r[8], r[9]};
  STAMP(2, 15);
  STAMP_END(2);
}

extern "C" __global__ void k9_end() {}

extern "C" void kernel_launch(void* const* d_in, const int* in_sizes, int n_in, void* d_out, int out_size,
                              void* d_ws, size_t ws_size, hipStream_t stream) {
  const unsigned* x = (const unsigned*)d_in[0];
  const unsigned short* wr = (const unsigned short*)d_in[1];
  const unsigned short* wi = (const unsigned short*)d_in[2];
  const unsigned short* wg = (const unsigned short*)d_in[4];
  const unsigned short* wu = (const unsigned short*)d_in[5];
  const unsigned short* wd = (const unsigned short*)d_in[6];
  char* ws = (char*)d_ws;
  const size_t MB = 1 << 20;
  f2* T1 = (f2*)(ws);
  f2* Wt = (f2*)(ws + 6 * MB);
  f2* T2 = (f2*)(ws + 12 * MB);
  u4* W1t = (u4*)(ws + 18 * MB);
  u4* W2t = (u4*)(ws + 22 * MB);
  unsigned* hbuf = (unsigned*)(ws + 24 * MB);
  u4* ln2T = (u4*)(ws + 27 * MB);
  u4* actT = (u4*)(ws + 30 * MB);
  unsigned* cnt = (unsigned*)(ws + 38 * MB);
  const int K2_LDS = 2 * 16 * EP * 8;
  (void)hipFuncSetAttribute((const void*)k2_col, hipFuncAttributeMaxDynamicSharedMemorySize, K2_LDS);
  (void)hipFuncSetAttribute((const void*)k3_mlp, hipFuncAttributeMaxDynamicSharedMemorySize, B_LDS);
  k1_rowfwd<<<384, 512, 0, stream>>>(x, (f4*)T1, wr, wi, (f4*)Wt, cnt SPASS);
  k2_col<<<256, 256, K2_LDS, stream>>>(T1, T2, Wt, wg, wu, wd, W1t, W2t SPASS);
  k3_mlp<<<256, 256, B_LDS, stream>>>((const f4*)T2, x, W1t, W2t, hbuf, ln2T, actT, (unsigned*)d_out, cnt SPASS);
}
```
